```python
import torch

def _softmax(x, axis=-1):
    return torch.softmax(x, dim=axis)
def _gelu(x):
    return torch.nn.functional.gelu(x, approximate="tanh")
def _silu(x):
    return torch.nn.functional.silu(x)
def _sigmoid(x):
    return torch.sigmoid(x)
def _layernorm(x, axis=-1, eps=1e-5):
    m = x.mean(dim=axis, keepdim=True)
    v = x.var(dim=axis, keepdim=True, unbiased=False)
    return (x - m) / torch.sqrt(v + eps)
def _scatter_add(tgt, idx, src, axis=0):
    return tgt.clone().index_add_(axis, idx, src)
def _segment_sum(src, seg, n):
    out = torch.zeros((n,) + src.shape[1:], dtype=src.dtype)
    return out.index_add_(0, seg, src)
def _pad(x, pw):
    flat = tuple(v for pair in reversed(pw) for v in pair)
    return torch.nn.functional.pad(x, flat)

kernel_name = 'parallel_sum__sliding_block_cached__geglu_ffn__decode'

def setup_inputs():

    _w = (lambda a: (lambda t: t.to(torch.bfloat16) if t.is_floating_point() else t)(torch.as_tensor(a)))
    def _A_setup():
        B, Sq, D = 2, 1, 640
        Hq, Hkv, Sk, Dh = 32, 32, 4096, 128
        assert Hq % Hkv == 0, (Hq, Hkv)
        return {
            "x": _w((lambda *s: torch.randn(*s, dtype=torch.bfloat16) * 0.05)(B, Sq, D)),
            "k_cache": _w((lambda *s: torch.randn(*s, dtype=torch.bfloat16) * 3.2)(B, Hkv, Sk, Dh)),
            "v_cache": _w((lambda *s: torch.randn(*s, dtype=torch.bfloat16) * 1.6)(B, Hkv, Sk, Dh)),
            "wq": _w((lambda *s: torch.randn(*s, dtype=torch.bfloat16) * 3.2)(D, Hq * Dh) * (D ** -0.5)),
            "wo": _w((lambda *s: torch.randn(*s, dtype=torch.bfloat16) * 1.6)(Hq * Dh, D) * ((Hq * Dh) ** -0.5)),
        }
    _A_in = _A_setup()

    def _B_setup():
        B, S, D, F = 2, 1, 640, 1728
        return {
            "x": _w((lambda *s: torch.randn(*s, dtype=torch.bfloat16) * 0.05)(B, S, D)),
            "w_gate": _w((lambda *s: torch.randn(*s, dtype=torch.bfloat16) * 0.05)(D, F)),
            "w_up": _w((lambda *s: torch.randn(*s, dtype=torch.bfloat16) * 0.05)(D, F)),
            "w_down": _w((lambda *s: torch.randn(*s, dtype=torch.bfloat16) * 0.05)(F, D)),
        }
    _B_in = _B_setup()

    return {
        "A_x": _A_in["x"],
        "A_k_cache": _A_in["k_cache"],
        "A_v_cache": _A_in["v_cache"],
        "A_wq": _A_in["wq"],
        "A_wo": _A_in["wo"],
        "B_x": _B_in["x"],
        "B_w_gate": _B_in["w_gate"],
        "B_w_up": _B_in["w_up"],
        "B_w_down": _B_in["w_down"],
    }

def reference(A_x, A_k_cache, A_v_cache, A_wq, A_wo, B_x, B_w_gate, B_w_up, B_w_down):
    def _A_ref(x, k_cache, v_cache, wq, wo):
        B, Sq, D = x.shape
        Hq, Dh = 32, 128
        Hkv, Sk = k_cache.shape[1], k_cache.shape[2]
        W = min(4096, max(256, Sk // 4))
        rep = Hq // Hkv
        q = (lambda x, *d: x.permute(*d))((x @ wq).reshape(B, Sq, Hq, Dh), 0, 2, 1, 3)
        k = (lambda x, n, axis: torch.repeat_interleave(x, n, dim=axis))(k_cache[:, :, Sk - W:, :], rep, axis=1)
        v = (lambda x, n, axis: torch.repeat_interleave(x, n, dim=axis))(v_cache[:, :, Sk - W:, :], rep, axis=1)
        scores = torch.einsum("bhqd,bhkd->bhqk", q, k) * (Dh ** -0.5)
        p = _softmax(scores, axis=-1)
        o = torch.einsum("bhqk,bhkd->bhqd", p, v)
        return (lambda x, *d: x.permute(*d))(o, 0, 2, 1, 3).reshape(B, Sq, Hq * Dh) @ wo

    def _B_ref(x, w_gate, w_up, w_down):
        return (_gelu(x @ w_gate) * (x @ w_up)) @ w_down

    x = A_x
    xn = _layernorm(x, axis=-1)
    return x + _A_ref(xn, A_k_cache, A_v_cache, A_wq, A_wo) + _B_ref(xn, B_w_gate, B_w_up, B_w_down)
```

```cpp
#include <hip/hip_runtime.h>
#include <hip/hip_bf16.h>
#include <stdint.h>
#define MIMIC 0
typedef unsigned short u16;
__device__ __forceinline__ float bf2f(u16 v) { return __uint_as_float((unsigned)v << 16); }
__device__ __forceinline__ u16 f2bf(float f) { unsigned u = __float_as_uint(f); u += 0x7FFFu + ((u >> 16) & 1u); return (u16)(u >> 16); }
__device__ __forceinline__ float rb(float f) { return MIMIC ? bf2f(f2bf(f)) : f; }

constexpr int D = 640, H = 32, DH = 128, SK = 4096, W = 1024, F = 1728;

__device__ void layernorm(const u16* x, float* xn, float* red) {
  int t = threadIdx.x;
  for (int b = 0; b < 2; ++b) {
    float s = 0.f;
    for (int i = t; i < D; i += blockDim.x) s += bf2f(x[b * D + i]);
    red[t] = s; __syncthreads();
    for (int o = blockDim.x / 2; o > 0; o >>= 1) { if (t < o) red[t] += red[t + o]; __syncthreads(); }
    float mean = red[0] / D; __syncthreads();
    float v = 0.f;
    for (int i = t; i < D; i += blockDim.x) { float d = bf2f(x[b * D + i]) - mean; v += d * d; }
    red[t] = v; __syncthreads();
    for (int o = blockDim.x / 2; o > 0; o >>= 1) { if (t < o) red[t] += red[t + o]; __syncthreads(); }
    float var = red[0] / D; __syncthreads();
    float m = rb(mean), vv = rb(var);
    float den = rb(sqrtf(rb(vv + 1e-5f)));
    for (int i = t; i < D; i += blockDim.x) xn[b * D + i] = rb(rb(bf2f(x[b * D + i]) - m) / den);
    __syncthreads();
  }
}

__global__ void __launch_bounds__(256) k1(const u16* x, const u16* wq, const u16* wg, const u16* wu, float* q, float* hbuf) {
  __shared__ float xn[2 * D]; __shared__ float red[256];
  layernorm(x, xn, red);
  int c = blockIdx.x * 256 + threadIdx.x;
  if (c < H * DH) {
    float a0 = 0, a1 = 0;
    for (int k = 0; k < D; ++k) { float w = bf2f(wq[k * (H * DH) + c]); a0 += xn[k] * w; a1 += xn[D + k] * w; }
    q[c] = rb(a0); q[H * DH + c] = rb(a1);
  } else if (c - H * DH < F) {
    int f = c - H * DH;
    float g0 = 0, g1 = 0, u0 = 0, u1 = 0;
    for (int k = 0; k < D; ++k) { float a = bf2f(wg[k * F + f]), b = bf2f(wu[k * F + f]); g0 += xn[k] * a; g1 += xn[D + k] * a; u0 += xn[k] * b; u1 += xn[D + k] * b; }
    g0 = rb(g0); g1 = rb(g1); u0 = rb(u0); u1 = rb(u1);
    auto gelu = [](float v) { return 0.5f * v * (1.f + tanhf(0.7978845608028654f * (v + 0.044715f * v * v * v))); };
    hbuf[f] = rb(rb(gelu(g0)) * u0); hbuf[F + f] = rb(rb(gelu(g1)) * u1);
  }
}

__global__ void __launch_bounds__(1024) k2(const u16* kc, const u16* vc, const float* q, float* o) {
  __shared__ float qs[DH]; __shared__ float p[W]; __shared__ float red[1024];
  int bh = blockIdx.x, t = threadIdx.x;
  if (t < DH) qs[t] = q[bh * DH + t];
  __syncthreads();
  const u16* kr = kc + ((size_t)bh * SK + (SK - W) + t) * DH;
  float s = 0;
  for (int d = 0; d < DH; ++d) s += qs[d] * bf2f(kr[d]);
  s = rb(rb(s) * rb(0.08838834764831845f));
  red[t] = s; __syncthreads();
  for (int of = 512; of > 0; of >>= 1) { if (t < of) red[t] = fmaxf(red[t], red[t + of]); __syncthreads(); }
  float m = red[0]; __syncthreads();
  float e = expf(s - m);
  red[t] = e; __syncthreads();
  for (int of = 512; of > 0; of >>= 1) { if (t < of) red[t] += red[t + of]; __syncthreads(); }
  float l = red[0]; __syncthreads();
  p[t] = rb(e / l); __syncthreads();
  int d = t & 127, g = t >> 7;
  float a = 0;
  for (int k = g * 128; k < g * 128 + 128; ++k) a += p[k] * bf2f(vc[((size_t)bh * SK + (SK - W) + k) * DH + d]);
  red[t] = a; __syncthreads();
  if (t < DH) { float r = 0; for (int i = 0; i < 8; ++i) r += red[i * 128 + t]; o[bh * DH + t] = rb(r); }
}

__global__ void __launch_bounds__(256) k3(const u16* x, const u16* wo, const u16* wd, const float* o, const float* hbuf, u16* out) {
  __shared__ float ra[256], rbb[256];
  int n = blockIdx.x % D, b = blockIdx.x / D, t = threadIdx.x;
  float a = 0, c = 0;
  for (int k = t; k < H * DH; k += 256) a += o[b * H * DH + k] * bf2f(wo[k * D + n]);
  for (int k = t; k < F; k += 256) c += hbuf[b * F + k] * bf2f(wd[k * D + n]);
  ra[t] = a; rbb[t] = c; __syncthreads();
  for (int of = 128; of > 0; of >>= 1) { if (t < of) { ra[t] += ra[t + of]; rbb[t] += rbb[t + of]; } __syncthreads(); }
  if (t == 0) out[b * D + n] = f2bf(rb(bf2f(x[b * D + n]) + rb(ra[0])) + rb(rbb[0]));
}

extern "C" void kernel_launch(void* const* d_in, const int* in_sizes, int n_in, void* d_out, int out_size, void* d_ws, size_t ws_size, hipStream_t stream) {
  const u16* x = (const u16*)d_in[0]; const u16* kc = (const u16*)d_in[1]; const u16* vc = (const u16*)d_in[2];
  const u16* wq = (const u16*)d_in[3]; const u16* wo = (const u16*)d_in[4];
  const u16* wg = (const u16*)d_in[6]; const u16* wu = (const u16*)d_in[7]; const u16* wd = (const u16*)d_in[8];
  float* ws = (float*)d_ws; float* q = ws; float* hb = ws + 8192; float* o = ws + 16384;
  k1<<<(H * DH + F + 255) / 256, 256, 0, stream>>>(x, wq, wg, wu, q, hb);
  k2<<<64, 1024, 0, stream>>>(kc, vc, q, o);
  k3<<<2 * D, 256, 0, stream>>>(x, wo, wd, o, hb, (u16*)d_out);
}
```

```cpp
#include <hip/hip_runtime.h>
#include <stdint.h>

#define GAS __attribute__((address_space(1)))
#define LAS __attribute__((address_space(3)))
typedef unsigned u32;
typedef unsigned long long u64;
typedef u32 v4u __attribute__((ext_vector_type(4)));
typedef u32 v2u __attribute__((ext_vector_type(2)));
typedef u32 v4u_a4 __attribute__((ext_vector_type(4), aligned(4)));
typedef u32 v2u_a4 __attribute__((ext_vector_type(2), aligned(4)));
typedef float f32x4 __attribute__((ext_vector_type(4)));
typedef float f32x2 __attribute__((ext_vector_type(2)));
typedef __bf16 bf16x2 __attribute__((ext_vector_type(2)));
typedef __bf16 bf16x8 __attribute__((ext_vector_type(8)));
typedef short s16x4 __attribute__((ext_vector_type(4)));

#define NT_KV 1
#define NT_W1 0
#define NT_W2 0
#define QSLEEP 16

constexpr int NTHR = 512;
constexpr int X1_OFF = 4096, X1_REC = 2048;
constexpr int X2_OFF = X1_OFF + 256 * X1_REC, X2_REC = 640;
constexpr int X3_OFF = X2_OFF + 256 * X2_REC, X3_UNIT = 4096;
constexpr int WS_BYTES = X3_OFF + 80 * X3_UNIT;
constexpr int AUX_SC1 = 16;
constexpr u32 SPIN_CAP = 1u << 16;

constexpr int L_W = 0;
constexpr int L_RED = 192 * 160;
constexpr int L_Q = L_RED + 4096;
constexpr int L_H = L_Q + 256;
constexpr int L_O = L_H + 256;
constexpr int L_ML = L_O + 2048;
constexpr int L_C = L_ML + 32;
constexpr int L_END = L_C + 32;
constexpr int LDS_BYTES = 84 * 1024;
enum { C_P1 = 0, F_Q = 1, F_H = 2, C_ISS = 3, C_DMA = 4, C_O = 5, F_PUB = 6 };

#define STAMP(n)
#define CNT(n, v) (void)(v)
#define CBAR() asm volatile("" ::: "memory")

template <int NT> __device__ __forceinline__ v4u ldg(const GAS char* p) {
  return NT ? __builtin_nontemporal_load((const GAS v4u*)p) : *(const GAS v4u*)p;
}
__device__ __forceinline__ u32 cvtpk(float lo, float hi) {
  return __builtin_bit_cast(u32, __builtin_convertvector(f32x2{lo, hi}, bf16x2));
}
__device__ __forceinline__ float rbf(float v) { return __builtin_bit_cast(float, cvtpk(0.f, v)); }
__device__ __forceinline__ float bflo(u32 v) { return __builtin_bit_cast(float, v << 16); }
__device__ __forceinline__ float bfhi(u32 v) { return __builtin_bit_cast(float, v & 0xFFFF0000u); }
__device__ __forceinline__ float dot2(u32 a, u32 b, float c) {
  return __builtin_amdgcn_fdot2_f32_bf16(__builtin_bit_cast(bf16x2, a), __builtin_bit_cast(bf16x2, b), c, false);
}
__device__ __forceinline__ f32x4 mfma(v4u a, v4u b, f32x4 c) {
  return __builtin_amdgcn_mfma_f32_16x16x32_bf16(__builtin_bit_cast(bf16x8, a), __builtin_bit_cast(bf16x8, b), c, 0, 0, 0);
}
template <int CTRL> __device__ __forceinline__ float dppf(float x) {
  return __builtin_bit_cast(float, __builtin_amdgcn_mov_dpp(__builtin_bit_cast(int, x), CTRL, 0xf, 0xf, true));
}
template <int CTRL> __device__ __forceinline__ u32 dppu(u32 x) { return (u32)__builtin_amdgcn_mov_dpp((int)x, CTRL, 0xf, 0xf, true); }
constexpr int XOR1 = 0xB1, XOR2 = 0x4E, XOR7 = 0x141, ROR8 = 0x128, ROR4 = 0x124, BCAST = 0x150;
__device__ __forceinline__ float sum16x(float x) {
  auto s = __builtin_amdgcn_permlane16_swap(__float_as_uint(x), __float_as_uint(x), false, false);
  return __uint_as_float(s[0]) + __uint_as_float(s[1]);
}
__device__ __forceinline__ float sum32x(float x) {
  auto s = __builtin_amdgcn_permlane32_swap(__float_as_uint(x), __float_as_uint(x), false, false);
  return __uint_as_float(s[0]) + __uint_as_float(s[1]);
}
__device__ __forceinline__ float max16x(float x) {
  auto s = __builtin_amdgcn_permlane16_swap(__float_as_uint(x), __float_as_uint(x), false, false);
  return fmaxf(__uint_as_float(s[0]), __uint_as_float(s[1]));
}
__device__ __forceinline__ float max32x(float x) {
  auto s = __builtin_amdgcn_permlane32_swap(__float_as_uint(x), __float_as_uint(x), false, false);
  return fmaxf(__uint_as_float(s[0]), __uint_as_float(s[1]));
}
__device__ __forceinline__ float rowsum(float x) { x += dppf<XOR1>(x); x += dppf<XOR2>(x); x += dppf<ROR4>(x); x += dppf<ROR8>(x); return x; }
__device__ __forceinline__ float rowmax(float x) { x = fmaxf(x, dppf<XOR1>(x)); x = fmaxf(x, dppf<XOR2>(x)); x = fmaxf(x, dppf<ROR4>(x)); x = fmaxf(x, dppf<ROR8>(x)); return x; }

__device__ __forceinline__ u32 lds_arrive(LAS char* L, int word, int lane) {
  u32 old = 0;
  CBAR();
  if (lane == 0) old = __hip_atomic_fetch_add((LAS u32*)(L + L_C) + word, 1u, __ATOMIC_RELAXED, __HIP_MEMORY_SCOPE_WORKGROUP);
  old = __builtin_amdgcn_readfirstlane(old);
  CBAR();
  return old;
}
__device__ __forceinline__ void lds_set(LAS char* L, int word) { CBAR(); *((volatile LAS u32*)(L + L_C) + word) = 1u; CBAR(); }
__device__ __forceinline__ void lds_wait(LAS char* L, int word, u32 want) {
#pragma nounroll
  for (u32 n = 0; n < (1u << 22); ++n) {
    CBAR();
    if (__builtin_amdgcn_readfirstlane(*((volatile LAS u32*)(L + L_C) + word)) >= want) break;
    __builtin_amdgcn_s_sleep(1);
  }
  CBAR();
}
__device__ __forceinline__ v4u ld_sc1(__amdgpu_buffer_rsrc_t rs, int off) { return __builtin_amdgcn_raw_buffer_load_b128(rs, off, 0, AUX_SC1); }
__device__ __forceinline__ void st_sc1(__amdgpu_buffer_rsrc_t rs, int off, v4u v) { __builtin_amdgcn_raw_buffer_store_b128(v, rs, off, 0, AUX_SC1); }
__device__ __forceinline__ bool tagok(v4u v, u32 tag) { return (v.y == tag) & (v.w == tag); }
template <int N> __device__ __forceinline__ u32 gather(__amdgpu_buffer_rsrc_t rs, const int (&off)[N], u32 tag, v4u (&v)[N]) {
  u32 n = 0;
#pragma nounroll
  for (; n < SPIN_CAP; ++n) {
    CBAR();
    bool ok = true;
#pragma unroll
    for (int k = 0; k < N; ++k) v[k] = ld_sc1(rs, off[k]);
#pragma unroll
    for (int k = 0; k < N; ++k) ok &= tagok(v[k], tag);
    if (__all(ok)) break;
  }
  CBAR();
  return n;
}
__device__ __forceinline__ v4u trfrag(LAS char* p) {
  typedef LAS s16x4* P;
  const v2u a = __builtin_bit_cast(v2u, __builtin_amdgcn_ds_read_tr16_b64_v4i16((P)p));
  const v2u b = __builtin_bit_cast(v2u, __builtin_amdgcn_ds_read_tr16_b64_v4i16((P)(p + 160)));
  return v4u{a.x, a.y, b.x, b.y};
}
__device__ __forceinline__ float gelu_tanh(float v) {
  const float z = 0.7978845608028654f * (v + 0.044715f * v * v * v);
  const float e = __builtin_amdgcn_exp2f(z * 2.885390081777927f);
  const float th = 1.f - 2.f * __builtin_amdgcn_rcpf(e + 1.f);
  return 0.5f * v * (1.f + th);
}

template <int T0, int NTILE>
__device__ __forceinline__ void out_proj(LAS char* L, __amdgpu_buffer_rsrc_t rs, u32 tag, int lane, int h, int j, bool ffn
) {
  const int g = lane >> 4, i = lane & 15;
  lds_wait(L, C_DMA, 2);
  STAMP(4);
  LAS char* wb = L + L_W + (2 * g + 8 * ((lane >> 2) & 3)) * 160 + (lane & 3) * 8 + T0 * 32;
  v4u bw[NTILE][4];
#pragma unroll
  for (int t = 0; t < NTILE; ++t)
#pragma unroll
    for (int ks = 0; ks < 4; ++ks) bw[t][ks] = trfrag(wb + ks * 32 * 160 + t * 32);
  float yf[NTILE];
#pragma unroll
  for (int t = 0; t < NTILE; ++t) yf[t] = 0.f;
  if (ffn) {
    lds_wait(L, F_H, 1);
    const v4u h0 = *(LAS v4u*)(L + L_H + ((i >> 2) & 1) * 128 + g * 16), h1 = *(LAS v4u*)(L + L_H + ((i >> 2) & 1) * 128 + 64 + g * 16);
#pragma unroll
    for (int t = 0; t < NTILE; ++t) {
      f32x4 d = mfma(h0, trfrag(wb + 4 * 32 * 160 + t * 32), f32x4{0.f, 0.f, 0.f, 0.f});
      d = mfma(h1, trfrag(wb + 5 * 32 * 160 + t * 32), d);
      yf[t] = d[0];
    }
  }
  lds_wait(L, F_PUB, 1);
  STAMP(5);
  const int base = X2_OFF + h * 8 * X2_REC;
  const int o0 = base + (i & 7) * X2_REC + (i >> 3) * 256 + g * 32;
  const int off[5] = {o0, o0 + 16, o0 + 128, o0 + 144, base + (4 * (g & 1) + (i & 3)) * X2_REC + 512};
  v4u v[5];
  CNT(11, gather<5>(rs, off, tag, v));
  STAMP(6);
  const v4u a0 = {v[0].x, v[0].z, v[1].x, v[1].z}, a1 = {v[2].x, v[2].z, v[3].x, v[3].z};
  const float m = __uint_as_float(v[4].x), l = __uint_as_float(v[4].z);
  float M = fmaxf(m, dppf<XOR1>(m)); M = fmaxf(M, dppf<XOR2>(M));
  const float e = __builtin_amdgcn_exp2f((m - M) * 1.4426950408889634f);
  float Ls = e * l; Ls += dppf<XOR1>(Ls); Ls += dppf<XOR2>(Ls);
  const float w = e * __builtin_amdgcn_rcpf(Ls);
  const float w0 = dppf<0x00>(w), w1 = dppf<0x55>(w), w2 = dppf<0xAA>(w), w3 = dppf<0xFF>(w);
#pragma unroll
  for (int t = 0; t < NTILE; ++t) {
    f32x4 lo = mfma(a0, bw[t][0], f32x4{0.f, 0.f, 0.f, 0.f});
    f32x4 hi = mfma(a0, bw[t][2], f32x4{0.f, 0.f, 0.f, 0.f});
    lo = mfma(a1, bw[t][1], lo);
    hi = mfma(a1, bw[t][3], hi);
    const f32x4 d = g < 2 ? lo : hi;
    float y = (d[0] * w0 + d[1] * w1) + (d[2] * w2 + d[3] * w3);
    y = sum32x(y) + yf[t];
    auto s = __builtin_amdgcn_permlane16_swap(__float_as_uint(y), __float_as_uint(y), false, false);
    if (lane < 16) st_sc1(rs, X3_OFF + (j * 10 + (T0 + t) * 2 + (i >> 3)) * X3_UNIT + h * 128 + (i & 7) * 16, v4u{s[0], tag, s[1], tag});
  }
  STAMP(7);
}

__global__ void __launch_bounds__(NTHR, 2)
fused_decode_kernel(const char* __restrict__ x_, const char* __restrict__ kc_, const char* __restrict__ vc_, const char* __restrict__ wq_,
                    const char* __restrict__ wo_, const char* __restrict__ wg_, const char* __restrict__ wu_, const char* __restrict__ wd_,
                    char* out_, char* ws_) {
  extern __shared__ __attribute__((aligned(16))) char smem[];
  LAS char* L = (LAS char*)smem;
  STAMP(0);
  asm volatile("" :: "s"(x_), "s"(kc_), "s"(vc_), "s"(wq_), "s"(wo_), "s"(wg_), "s"(wu_), "s"(wd_), "s"(out_), "s"(ws_));
  const GAS char* x = (const GAS char*)x_;
  const int lane = threadIdx.x & 63, wave = __builtin_amdgcn_readfirstlane(threadIdx.x >> 6);
  const int xcd = blockIdx.x & 7, r = blockIdx.x >> 3, hl = r >> 3, h = xcd * 4 + hl, j = r & 7;
  const bool ffn = hl < 3 || xcd < 3;
  const int fb = hl < 3 ? xcd * 3 + hl : 24 + xcd;
  const int g = lane >> 4, i = lane & 15;
  const __amdgpu_buffer_rsrc_t rs = __builtin_amdgcn_make_buffer_rsrc((void*)ws_, 0, WS_BYTES, 0x00020000);
  const u32 half = (i & 1) ? 0xFFFF0000u : 0x0000FFFFu;
  const int pos = (i & 7) >> 1;
  const u32 mk0 = pos == 0 ? half : 0u, mk1 = pos == 1 ? half : 0u, mk2 = pos == 2 ? half : 0u, mk3 = pos == 3 ? half : 0u;

  if (wave < 4) {
    const int kq = j & 3, ch = j >> 2, row0 = kq * 160 + wave * 40 + g * 10;
    const GAS char* xs = x + (lane >> 5) * 1280 + (lane & 31) * 40;
    const v4u xa = *(const GAS v4u_a4*)xs, xb = *(const GAS v4u_a4*)(xs + 16);
    const v2u xc = *(const GAS v2u_a4*)(xs + 32);
    const GAS char* xr = x + ((i >> 3) * 640 + row0) * 2;
    const v4u xd = *(const GAS v4u_a4*)xr;
    const u32 xe = *(const GAS u32*)(xr + 16);
    const u32 ep = __builtin_amdgcn_raw_buffer_load_b32(rs, 0, 0, AUX_SC1);
    const bool m1 = (i >> 3) && ffn;
    const GAS char* wp = m1 ? (const GAS char*)(ch ? wu_ : wg_) + row0 * 3456 + fb * 128 + (i & 7) * 16
                            : (const GAS char*)wq_ + row0 * 8192 + h * 256 + ch * 128 + (i & 7) * 16;
    const int stride = m1 ? 3456 : 8192;
    v4u a[10];
#pragma unroll
    for (int t = 0; t < 10; ++t) a[t] = ldg<NT_W1>(wp + t * stride);
    __builtin_amdgcn_sched_barrier(0);
    STAMP(9);
    if (wave == 0 && lane < 8) *((volatile LAS u32*)(L + L_C) + lane) = 0u;
    asm volatile("s_waitcnt lgkmcnt(0)" ::: "memory");
    __builtin_amdgcn_s_barrier();
    CBAR();
    const u32 one2 = 0x3F803F80u;
    float s1 = 0.f, s2 = 0.f;
#pragma unroll
    for (int k = 0; k < 4; ++k) { s1 = dot2(xa[k], one2, s1); s2 = dot2(xa[k], xa[k], s2); s1 = dot2(xb[k], one2, s1); s2 = dot2(xb[k], xb[k], s2); }
#pragma unroll
    for (int k = 0; k < 2; ++k) { s1 = dot2(xc[k], one2, s1); s2 = dot2(xc[k], xc[k], s2); }
    s1 = sum16x(rowsum(s1)); s2 = sum16x(rowsum(s2));
    auto p1 = __builtin_amdgcn_permlane32_swap(__float_as_uint(s1), __float_as_uint(s1), false, false);
    auto p2 = __builtin_amdgcn_permlane32_swap(__float_as_uint(s2), __float_as_uint(s2), false, false);
    const float mean = __uint_as_float((i >> 3) ? p1[1] : p1[0]) * (1.f / 640.f);
    const float var = fmaxf(__uint_as_float((i >> 3) ? p2[1] : p2[0]) * (1.f / 640.f) - mean * mean, 0.f);
    const float mb = rbf(mean);
    const float rden = 1.f / rbf(__builtin_sqrtf(rbf(rbf(var) + 1e-5f)));
    const u32 tag = __builtin_amdgcn_readfirstlane(ep) + 1u;
    STAMP(1);
    f32x4 acc = {0.f, 0.f, 0.f, 0.f};
#pragma unroll
    for (int t = 0; t < 10; ++t) {
      const u32 xw = t < 8 ? xd[t >> 1] : xe;
      const float xv = (t & 1) ? bfhi(xw) : bflo(xw);
      const float n = rbf(xv - mb) * rden;
      const u32 d = cvtpk(n, n);
      acc = mfma(a[t], v4u{d & mk0, d & mk1, d & mk2, d & mk3}, acc);
    }
    *(LAS f32x4*)(L + L_RED + wave * 1024 + lane * 16) = acc;
    STAMP(8);
    if (lds_arrive(L, C_P1, lane) == 3u) {
      f32x4 s = *(LAS f32x4*)(L + L_RED + lane * 16);
#pragma unroll
      for (int w = 1; w < 4; ++w) s += *(LAS f32x4*)(L + L_RED + w * 1024 + lane * 16);
      const int o = X1_OFF + (h * 8 + j) * X1_REC + lane * 32;
      st_sc1(rs, o, v4u{__float_as_uint(s[0]), tag, __float_as_uint(s[1]), tag});
      st_sc1(rs, o + 16, v4u{__float_as_uint(s[2]), tag, __float_as_uint(s[3]), tag});
    }
    STAMP(2);
    const int x1g = X1_OFF + h * 8 * X1_REC;
    if (wave == 0) {
      __builtin_amdgcn_s_sleep(QSLEEP);
      const int cq = lane >> 5, gq = (lane >> 4) & 1, c7 = (lane >> 1) & 7, u = lane & 1;
      const int o = x1g + cq * 4 * X1_REC + (gq * 16 + (j >> 2) * 8 + c7) * 32 + u * 16;
      const int off[4] = {o, o + X1_REC, o + 2 * X1_REC, o + 3 * X1_REC};
      v4u v[4];
      CNT(10, gather<4>(rs, off, tag, v));
      const float q0 = (__uint_as_float(v[0].x) + __uint_as_float(v[1].x)) + (__uint_as_float(v[2].x) + __uint_as_float(v[3].x));
      const float q1 = (__uint_as_float(v[0].z) + __uint_as_float(v[1].z)) + (__uint_as_float(v[2].z) + __uint_as_float(v[3].z));
      const u32 pk = cvtpk(q0, q1);
      LAS unsigned short* qd = (LAS unsigned short*)(L + L_Q) + cq * 64 + gq * 32 + u * 16 + c7;
      qd[0] = (unsigned short)pk;
      qd[8] = (unsigned short)(pk >> 16);
      lds_set(L, F_Q);
      STAMP(3);
      out_proj<0, 3>(L, rs, tag, lane, h, j, ffn
      );
    } else if (wave == 1) {
      if (ffn) {
        lds_wait(L, F_Q, 1);
        const int o = x1g + 1024 + lane * 16;
        const int off[8] = {o, o + X1_REC, o + 2 * X1_REC, o + 3 * X1_REC, o + 4 * X1_REC, o + 5 * X1_REC, o + 6 * X1_REC, o + 7 * X1_REC};
        v4u v[8];
        CNT(10, gather<8>(rs, off, tag, v));
        const float g0 = (__uint_as_float(v[0].x) + __uint_as_float(v[1].x)) + (__uint_as_float(v[2].x) + __uint_as_float(v[3].x));
        const float g1 = (__uint_as_float(v[0].z) + __uint_as_float(v[1].z)) + (__uint_as_float(v[2].z) + __uint_as_float(v[3].z));
        const float u0 = (__uint_as_float(v[4].x) + __uint_as_float(v[5].x)) + (__uint_as_float(v[6].x) + __uint_as_float(v[7].x));
        const float u1 = (__uint_as_float(v[4].z) + __uint_as_float(v[5].z)) + (__uint_as_float(v[6].z) + __uint_as_float(v[7].z));
        const float h0 = rbf(gelu_tanh(rbf(g0))) * rbf(u0), h1 = rbf(gelu_tanh(rbf(g1))) * rbf(u1);
        *(LAS u32*)(L + L_H + ((lane >> 4) & 1) * 128 + ((lane >> 5) * 16 + (lane & 15)) * 4) = cvtpk(h0, h1);
        lds_set(L, F_H);
      }
      STAMP(3);
      out_proj<3, 2>(L, rs, tag, lane, h, j, ffn
      );
    } else {
      int vo[5];
#pragma unroll
      for (int n = 0; n < 5; ++n) { const int e = n * 64 + lane, rr = e / 10; vo[n] = rr * 1280 + (e - rr * 10) * 16 + j * 160; }
      const int dw = wave - 2;
      lds_wait(L, C_ISS, 4);
      STAMP(3);
      if (ffn) {
        const GAS char* src = (const GAS char*)wd_ + (fb * 64 + dw * 32) * 1280;
#pragma unroll
        for (int n = 0; n < 5; ++n)
          __builtin_amdgcn_global_load_lds((const GAS void*)(src + vo[n]), (LAS void*)(L + L_W + (4 + dw) * 5120 + n * 1024), 16, 0, NT_W2 ? 2 : 0);
      }
#pragma unroll
      for (int q = 0; q < 2; ++q) {
        const GAS char* src = (const GAS char*)wo_ + (h * 128 + (dw * 2 + q) * 32) * 1280;
#pragma unroll
        for (int n = 0; n < 5; ++n)
          __builtin_amdgcn_global_load_lds((const GAS void*)(src + vo[n]), (LAS void*)(L + L_W + (dw * 2 + q) * 5120 + n * 1024), 16, 0, NT_W2 ? 2 : 0);
      }
      STAMP(4);
      asm volatile("s_waitcnt vmcnt(0)" ::: "memory");
      lds_arrive(L, C_DMA, lane);
      STAMP(5);
      if (wave == 2 && h < 10) {
        const int col = j * 80 + h * 8 + (lane & 7), ob = ((lane >> 3) & 1) * 640 + col;
        const float xv = bflo(*(const GAS unsigned short*)(x + ob * 2));
        const int o = X3_OFF + (j * 10 + h) * X3_UNIT + lane * 16;
        const int off[4] = {o, o + 1024, o + 2048, o + 3072};
        v4u v[4];
        CNT(11, gather<4>(rs, off, tag, v));
        STAMP(6);
        float y0 = (__uint_as_float(v[0].x) + __uint_as_float(v[1].x)) + (__uint_as_float(v[2].x) + __uint_as_float(v[3].x));
        float y1 = (__uint_as_float(v[0].z) + __uint_as_float(v[1].z)) + (__uint_as_float(v[2].z) + __uint_as_float(v[3].z));
        y0 += dppf<ROR8>(y0); y1 += dppf<ROR8>(y1);
        float y = (lane & 8) ? y1 : y0;
        y = sum32x(sum16x(y)) + xv;
        if (lane < 16) *(GAS unsigned short*)((GAS char*)out_ + ob * 2) = (unsigned short)cvtpk(y, y);
        if (blockIdx.x == 0 && lane == 0) __builtin_amdgcn_raw_buffer_store_b32(tag, rs, 0, 0, AUX_SC1);
        STAMP(7);
      }
    }
  } else {
    const int aw = wave - 4;
    const size_t kvo = ((size_t)((j >> 2) * 32 + h) * 4096 + 3072 + (j & 3) * 256 + aw * 64) * 256 + lane * 16;
    const GAS char* kp = (const GAS char*)kc_ + kvo;
    const GAS char* vp = (const GAS char*)vc_ + kvo;
    const u32 ep = __builtin_amdgcn_raw_buffer_load_b32(rs, 0, 0, AUX_SC1);
    __builtin_amdgcn_s_barrier();
    CBAR();
    STAMP(1);
    v4u kk[16], vv[16];
#pragma unroll
    for (int t = 0; t < 16; ++t) kk[t] = ldg<NT_KV>(kp + t * 1024);
#pragma unroll
    for (int t = 0; t < 16; ++t) vv[t] = ldg<NT_KV>(vp + t * 1024);
    __builtin_amdgcn_sched_barrier(0);
    lds_arrive(L, C_ISS, lane);
    STAMP(2);
    const u32 tag = __builtin_amdgcn_readfirstlane(ep) + 1u;
    lds_wait(L, F_Q, 1);
    STAMP(3);
    const v4u q = *(LAS v4u*)(L + L_Q + i * 16);
    float sx[16];
#pragma unroll
    for (int t = 0; t < 16; ++t) sx[t] = dot2(kk[t].w, q.w, dot2(kk[t].z, q.z, dot2(kk[t].y, q.y, dot2(kk[t].x, q.x, 0.f))));
#define TR_STEP(HALF, CTRL, BIT) _Pragma("unroll") for (int n = 0; n < (HALF); ++n) { \
      const float lo_ = sx[n], hi_ = sx[n + (HALF)]; const float keep_ = (i & (BIT)) ? hi_ : lo_, send_ = (i & (BIT)) ? lo_ : hi_; \
      sx[n] = keep_ + dppf<CTRL>(send_); }
    TR_STEP(8, ROR8, 8) TR_STEP(4, XOR7, 4) TR_STEP(2, XOR2, 2) TR_STEP(1, XOR1, 1)
#undef TR_STEP
    const float sc = rbf(rbf(sx[0]) * 0.08838834764831845f);
    const float m = max32x(max16x(rowmax(sc)));
    const float e = __builtin_amdgcn_exp2f((sc - m) * 1.4426950408889634f);
    const float l = sum32x(sum16x(rowsum(e)));
    const u32 pd = cvtpk(e, e);
    if (lane == 0) *(LAS f32x2*)(L + L_ML + aw * 8) = f32x2{m, l};
    STAMP(4);
    f32x4 acc = {0.f, 0.f, 0.f, 0.f};
#define PV(t) { const u32 d = dppu<BCAST + t>(pd); acc = mfma(vv[t], v4u{d & mk0, d & mk1, d & mk2, d & mk3}, acc); }
    PV(0) PV(1) PV(2) PV(3) PV(4) PV(5) PV(6) PV(7) PV(8) PV(9) PV(10) PV(11) PV(12) PV(13) PV(14) PV(15)
#undef PV
    if (i < 8) *(LAS f32x4*)(L + L_O + aw * 512 + (g * 8 + i) * 16) = acc;
    STAMP(5);
    if (lds_arrive(L, C_O, lane) == 3u) {
      const f32x4 ml0 = *(LAS f32x4*)(L + L_ML), ml1 = *(LAS f32x4*)(L + L_ML + 16);
      f32x4 o[4];
#pragma unroll
      for (int w = 0; w < 4; ++w) o[w] = *(LAS f32x4*)(L + L_O + w * 512 + (lane & 31) * 16);
      const float M = fmaxf(fmaxf(ml0[0], ml0[2]), fmaxf(ml1[0], ml1[2]));
      const float e0 = __builtin_amdgcn_exp2f((ml0[0] - M) * 1.4426950408889634f), e1 = __builtin_amdgcn_exp2f((ml0[2] - M) * 1.4426950408889634f);
      const float e2 = __builtin_amdgcn_exp2f((ml1[0] - M) * 1.4426950408889634f), e3 = __builtin_amdgcn_exp2f((ml1[2] - M) * 1.4426950408889634f);
      const float Ls = (e0 * ml0[1] + e1 * ml0[3]) + (e2 * ml1[1] + e3 * ml1[3]);
      const f32x4 s = (o[0] * e0 + o[1] * e1) + (o[2] * e2 + o[3] * e3);
      const u32 d0 = lane < 32 ? cvtpk(s[0], s[1]) : __float_as_uint(M), d1 = lane < 32 ? cvtpk(s[2], s[3]) : __float_as_uint(Ls);
      if (lane < 33) st_sc1(rs, X2_OFF + (h * 8 + j) * X2_REC + lane * 16, v4u{d0, tag, d1, tag});
      lds_set(L, F_PUB);
      STAMP(6);
    }
  }
}

extern "C" void kernel_launch(void* const* d_in, const int* in_sizes, int n_in, void* d_out, int out_size, void* d_ws, size_t ws_size,
                              hipStream_t stream) {
  (void)hipFuncSetAttribute((const void*)fused_decode_kernel, hipFuncAttributeMaxDynamicSharedMemorySize, LDS_BYTES);
  fused_decode_kernel<<<256, NTHR, LDS_BYTES, stream>>>(
      (const char*)d_in[0], (const char*)d_in[1], (const char*)d_in[2], (const char*)d_in[3], (const char*)d_in[4],
      (const char*)d_in[6], (const char*)d_in[7], (const char*)d_in[8], (char*)d_out, (char*)d_ws);
}
```

```cpp
#include <hip/hip_runtime.h>
#include <stdint.h>

#define GAS __attribute__((address_space(1)))
#define LAS __attribute__((address_space(3)))
typedef unsigned u32;
typedef unsigned long long u64;
typedef u32 v4u __attribute__((ext_vector_type(4)));
typedef u32 v2u __attribute__((ext_vector_type(2)));
typedef u32 v4u_a4 __attribute__((ext_vector_type(4), aligned(4)));
typedef u32 v2u_a4 __attribute__((ext_vector_type(2), aligned(4)));
typedef float f32x4 __attribute__((ext_vector_type(4)));
typedef float f32x2 __attribute__((ext_vector_type(2)));
typedef __bf16 bf16x2 __attribute__((ext_vector_type(2)));
typedef __bf16 bf16x8 __attribute__((ext_vector_type(8)));
typedef u64 v8l __attribute__((ext_vector_type(8)));
typedef u64 v2l __attribute__((ext_vector_type(2)));
typedef short s16x4 __attribute__((ext_vector_type(4)));

struct Sched { int first, d1, sk, dk, sv, sw2, d2, qg, hg, x2g, x3g, sy, x3s, nom, alpha, dv; };
constexpr Sched make_sched(const int* p) {
  Sched c = {};
  c.first = p[0]; c.d1 = p[1]; c.dk = p[3]; c.dv = p[15]; c.d2 = p[6];
  c.sk = p[0] + 8 * p[1] + p[2]; c.sv = c.sk + 16 * p[3] + p[4]; c.sw2 = c.sv + 16 * p[15] + p[5];
  c.qg = c.sk + p[7]; c.hg = c.sk + p[8]; c.x2g = c.sv + 15 * p[15] + p[9]; c.x3g = c.sw2 + p[10];
  c.sy = p[11]; c.x3s = p[12]; c.nom = p[13]; c.alpha = p[14];
  return c;
}
constexpr int kParams[16] = {272, 191, 100, 225, 92, 320, 189, 3840, 4800, 2480, 3365, 2220, 100, 1930, 4, 221};
constexpr Sched SC = make_sched(kParams);

constexpr int NTHR = 768;
constexpr u32 SPIN_CAP = 1u << 20;
constexpr int AUX_SC1 = 16;

constexpr int XT_OFF = 256;
constexpr int XS_OFF = 512;
constexpr int XP_OFF = 768;
constexpr int XH_OFF = 1280;
constexpr int X1_OFF = 16384;
constexpr int X2_OFF = 262144;
constexpr int X3_OFF = 524288;
constexpr int WS_BYTES = X3_OFF + 15 * 16384;
__device__ __forceinline__ int line_off(int n) { return X3_OFF + ((n >> 6) << 14) + ((n & 63) << 7); }

constexpr int L_W = 0;
constexpr int L_RED = 32768;
constexpr int L_Y = L_RED + 4096;
constexpr int L_Q = L_Y + 2048;
constexpr int L_O = L_Q + 256;
constexpr int L_ML = L_O + 3584;
constexpr int L_C = L_ML + 64;
constexpr int L_SPARE = 45056;
constexpr int LDS_BYTES = L_SPARE + 8192;
enum { C_P1 = L_C, F_Q = L_C + 4, C_ML = L_C + 8, C_O = L_C + 12, C_O2 = L_C + 16, V_TAG = L_C + 20, V_ORG = L_C + 32 };
static_assert(V_ORG + 4 <= L_SPARE, "LDS map");

#define CBAR() asm volatile("" ::: "memory")

__device__ __forceinline__ v4u ldnt(const GAS char* p) { return __builtin_nontemporal_load((const GAS v4u*)p); }
__device__ __forceinline__ u32 cvtpk(float lo, float hi) { return __builtin_bit_cast(u32, __builtin_convertvector(f32x2{lo, hi}, bf16x2)); }
__device__ __forceinline__ float rbf(float v) { return __builtin_bit_cast(float, cvtpk(0.f, v)); }
__device__ __forceinline__ float bflo(u32 v) { return __builtin_bit_cast(float, v << 16); }
__device__ __forceinline__ float bfhi(u32 v) { return __builtin_bit_cast(float, v & 0xFFFF0000u); }
__device__ __forceinline__ float asf(u32 v) { return __builtin_bit_cast(float, v); }
__device__ __forceinline__ u32 asu(float v) { return __builtin_bit_cast(u32, v); }
__device__ __forceinline__ float dot2(u32 a, u32 b, float c) {
  return __builtin_amdgcn_fdot2_f32_bf16(__builtin_bit_cast(bf16x2, a), __builtin_bit_cast(bf16x2, b), c, false);
}
__device__ __forceinline__ f32x4 mfma(v4u a, v4u b, f32x4 c) {
  return __builtin_amdgcn_mfma_f32_16x16x32_bf16(__builtin_bit_cast(bf16x8, a), __builtin_bit_cast(bf16x8, b), c, 0, 0, 0);
}
template <int CTRL> __device__ __forceinline__ u32 dppu(u32 x) { return (u32)__builtin_amdgcn_mov_dpp((int)x, CTRL, 0xf, 0xf, true); }
template <int CTRL> __device__ __forceinline__ float dppf(float x) { return asf(dppu<CTRL>(asu(x))); }
constexpr int XOR1 = 0xB1, XOR2 = 0x4E, XOR7 = 0x141, ROR8 = 0x128, ROR4 = 0x124, BCAST = 0x150;
__device__ __forceinline__ float add16(float a, float b) { auto s = __builtin_amdgcn_permlane16_swap(asu(a), asu(b), false, false); return asf(s[0]) + asf(s[1]); }
__device__ __forceinline__ float add32(float a, float b) { auto s = __builtin_amdgcn_permlane32_swap(asu(a), asu(b), false, false); return asf(s[0]) + asf(s[1]); }
__device__ __forceinline__ float max16(float a) { auto s = __builtin_amdgcn_permlane16_swap(asu(a), asu(a), false, false); return fmaxf(asf(s[0]), asf(s[1])); }
__device__ __forceinline__ float max32(float a) { auto s = __builtin_amdgcn_permlane32_swap(asu(a), asu(a), false, false); return fmaxf(asf(s[0]), asf(s[1])); }
__device__ __forceinline__ float rowsum(float x) { x += dppf<XOR1>(x); x += dppf<XOR2>(x); x += dppf<ROR4>(x); x += dppf<ROR8>(x); return x; }

template <int OFF> __device__ __forceinline__ u32 lds_arrive() {
  u32 old, z = 0, one = 1;
  asm volatile("s_mov_b64 exec, 1\n\tds_add_rtn_u32 %0, %1, %2 offset:%3\n\ts_mov_b64 exec, -1\n\ts_waitcnt lgkmcnt(0)"
               : "=&v"(old) : "v"(z), "v"(one), "i"(OFF) : "memory");
  return __builtin_amdgcn_readfirstlane(old);
}
template <int OFF> __device__ __forceinline__ void lds_signal() {
  u32 z = 0, one = 1;
  asm volatile("s_mov_b64 exec, 1\n\tds_add_u32 %0, %1 offset:%2\n\ts_mov_b64 exec, -1" :: "v"(z), "v"(one), "i"(OFF) : "memory");
}
template <int OFF, int WANT> __device__ __forceinline__ void lds_wait() {
  u32 v, z = 0;
  asm volatile("1:\n\tds_read_b32 %0, %1 offset:%2\n\ts_waitcnt lgkmcnt(0)\n\tv_cmp_gt_u32 vcc, %3, %0\n\ts_cbranch_vccnz 1b"
               : "=&v"(v) : "v"(z), "i"(OFF), "i"(WANT) : "memory", "vcc");
}
__device__ __forceinline__ void pace(u32 due) {
  __builtin_amdgcn_sched_barrier(0);
  asm volatile("1:\n\ts_memrealtime s[90:91]\n\ts_waitcnt lgkmcnt(0)\n\ts_sub_u32 s90, s90, %0\n\ts_cmp_lt_i32 s90, 0\n\ts_cbranch_scc1 1b"
               :: "s"(due) : "memory", "scc", "s90", "s91");
  __builtin_amdgcn_sched_barrier(0);
}

__device__ __forceinline__ void pace_org(u32 rel) {
  u32 v, z = 0;
  __builtin_amdgcn_sched_barrier(0);
  asm volatile("1:\n\tds_read_b32 %0, %1 offset:%3\n\ts_memrealtime s[90:91]\n\ts_waitcnt lgkmcnt(0)\n\tv_sub_u32 %0, s90, %0\n\tv_cmp_gt_i32 vcc, %2, %0\n\ts_cbranch_vccnz 1b"
               : "=&v"(v) : "v"(z), "s"(rel), "i"(V_ORG) : "memory", "vcc", "s90", "s91");
  __builtin_amdgcn_sched_barrier(0);
}

template <int FLAG> __device__ __forceinline__ u32 flag_or_time(u32 rel) {
  u32 v, f, z = 0;
  __builtin_amdgcn_sched_barrier(0);
  asm volatile("1:\n\tds_read_b32 %0, %2 offset:%4\n\tds_read_b32 %1, %2 offset:%5\n\ts_memrealtime s[90:91]\n\ts_waitcnt lgkmcnt(0)\n\t"
               "v_cmp_ne_u32 vcc, 0, %1\n\ts_cbranch_vccnz 2f\n\tv_sub_u32 %0, s90, %0\n\tv_cmp_gt_i32 vcc, %3, %0\n\ts_cbranch_vccnz 1b\n2:"
               : "=&v"(v), "=&v"(f) : "v"(z), "s"(rel), "i"(V_ORG), "i"(FLAG) : "memory", "vcc", "s90", "s91");
  __builtin_amdgcn_sched_barrier(0);
  return __builtin_amdgcn_readfirstlane(f);
}

__device__ __forceinline__ v4u ld_sc1(__amdgpu_buffer_rsrc_t rs, int off) { return __builtin_amdgcn_raw_buffer_load_b128(rs, off, 0, AUX_SC1); }
__device__ __forceinline__ void st_sc1(__amdgpu_buffer_rsrc_t rs, int off, v4u v) { __builtin_amdgcn_raw_buffer_store_b128(v, rs, off, 0, AUX_SC1); }
__device__ __forceinline__ u32 f24(float v, u32 tb) { return ((asu(v) + 0x80u) & 0xFFFFFF00u) | tb; }
__device__ __forceinline__ u32 f28(float v, u32 tb) { return ((asu(v) + 8u) & 0xFFFFFFF0u) | (tb & 0xFu); }
__device__ __forceinline__ v4u pack_f(f32x4 v, u32 tag) {
  return v4u{f28(v[0], tag), f28(v[1], tag >> 8), f28(v[2], tag >> 16), f28(v[3], tag >> 24)};
}
template <int N> __device__ __forceinline__ u32 gather_f(__amdgpu_buffer_rsrc_t rs, int off, int step, u32 tag, f32x4 (&v)[N]) {
  const v4u tb = {tag & 0xF, (tag >> 8) & 0xF, (tag >> 16) & 0xF, (tag >> 24) & 0xF};
  u32 n = 0;
#pragma nounroll
  for (; n < SPIN_CAP; ++n) {
    CBAR();
    v4u w[N];
    u32 bad = 0;
#pragma unroll
    for (int k = 0; k < N; ++k) w[k] = ld_sc1(rs, off + k * step);
#pragma unroll
    for (int k = 0; k < N; ++k) { w[k] ^= tb; bad |= (w[k].x | w[k].y) | (w[k].z | w[k].w); v[k] = __builtin_bit_cast(f32x4, w[k]); }
    if (__builtin_amdgcn_ballot_w64((bad & 0xFu) != 0u) == 0ull) break;
  }
  CBAR();
  return n;
}
__device__ __forceinline__ v4u trfrag(LAS char* p) {
  typedef LAS s16x4* P;
  const v2u a = __builtin_bit_cast(v2u, __builtin_amdgcn_ds_read_tr16_b64_v4i16((P)p));
  const v2u b = __builtin_bit_cast(v2u, __builtin_amdgcn_ds_read_tr16_b64_v4i16((P)(p + 256)));
  return v4u{a.x, a.y, b.x, b.y};
}
#define DMA4(src, dst, mask) asm volatile("s_mov_b64 exec, %6\n\ts_mov_b32 m0, %5\n\ts_nop 0\n\tglobal_load_lds_dwordx4 %0, %4 nt\n\t" \
        "s_add_u32 m0, m0, 0x400\n\ts_nop 0\n\tglobal_load_lds_dwordx4 %1, %4 nt\n\t" \
        "s_add_u32 m0, m0, 0x400\n\ts_nop 0\n\tglobal_load_lds_dwordx4 %2, %4 nt\n\t" \
        "s_add_u32 m0, m0, 0x400\n\ts_nop 0\n\tglobal_load_lds_dwordx4 %3, %4 nt\n\ts_mov_b64 exec, -1" \
        :: "v"(vo[0]), "v"(vo[1]), "v"(vo[2]), "v"(vo[3]), "s"(src), "s"(dst), "s"(mask) : "memory", "scc")
__device__ __forceinline__ float gelu_tanh(float v) {
  const float z = 0.7978845608028654f * (v + 0.044715f * v * v * v);
  return v * __builtin_amdgcn_rcpf(1.f + __builtin_amdgcn_exp2f(z * -2.885390081777927f));
}
constexpr float LOG2E = 1.4426950408889634f;

__global__ void __launch_bounds__(NTHR, 3)
fused_decode_kernel(const char*, const char*, const char*, const char*, const char*, const char*, const char*, const char*, char*, char*) {
  extern __shared__ __attribute__((aligned(16))) char smem[];
  LAS char* L = (LAS char*)smem;
  v8l ka; v2l kb; u32 wv;
  asm volatile("s_load_dwordx16 %0, %3, 0x0\n\ts_load_dwordx4 %1, %3, 0x40\n\t"
               "v_readfirstlane_b32 %2, v0\n\ts_lshr_b32 %2, %2, 6\n\ts_cmp_lt_u32 %2, 4\n\ts_cbranch_scc1 2f\n\t"
               "s_cmp_gt_u32 %2, 5\n\ts_cbranch_scc1 1f\n\t"
               "s_getpc_b64 s[90:91]\n.Lcode_begin:\n\tv_lshlrev_b32 v1, 7, v0\n\tv_add_u32 v1, 0xffff8400, v1\n\tv_min_u32 v1, .Lcode_end-.Lcode_begin, v1\n\t"
               "global_load_dword v1, v1, s[90:91]\n\ts_waitcnt vmcnt(0)\n"
               "1:\n\ts_waitcnt lgkmcnt(0)\n2:"
               : "=&s"(ka), "=&s"(kb), "=&s"(wv) : "s"(__builtin_amdgcn_kernarg_segment_ptr()) : "scc", "memory", "v1", "s90", "s91");
  __builtin_amdgcn_sched_barrier(0);
  const int wave = wv;
#define ARGS() asm volatile("s_waitcnt lgkmcnt(0)" : "+s"(ka), "+s"(kb))
#define IDS() const int lane = tid & 63, xcd = bid & 7, r = bid >> 3, h = xcd * 4 + (r >> 3), j = r & 7, g = lane >> 4, i = lane & 15
#define RSRC() const __amdgpu_buffer_rsrc_t rs = __builtin_amdgcn_make_buffer_rsrc((void*)kb[1], 0, WS_BYTES, 0x00020000)

  if (__builtin_expect(wave < 4, 1)) {
    const int tid = threadIdx.x, bid = blockIdx.x;
    IDS();
    const int n = r >> 1, slot = xcd * 7 + n - 9;
    const bool gu = n > 8, part = n == 3 || n == 8;
    u32 rw = (((u32)((xcd * 16 + (gu ? n : n == 8 ? 1 : n >> 1)) * 2 + (r & 1)) * 2654435761u) >> 16) * 10u >> 16;
    const u32 b0 = rw + (wave >> 1);
    const u32 stride = !gu ? 8192u : slot < 54 ? 3456u : 0u;
    const int sl = part ? n >> 3 : i >> 3;
    const int row0 = (r & 1) * 320 + min(b0, b0 - 10) * 32 + (wave & 1) * 16 + g * 4;
    const u32 col = (gu ? min(slot >> 1, 26) : xcd * 8 + (n == 8 ? 3 : n)) * 128 + (i & 7) * 16;
    u32 w0 = col + (row0 + sl * 2) * stride, w1 = w0 + stride;
    asm volatile("" : "+v"(w0), "+v"(w1));
    __builtin_amdgcn_sched_barrier(0);
    ARGS();
    const GAS char* wp = (const GAS char*)(!gu ? ka[3] : (slot & 1) ? ka[6] : ka[5]);
    v4u a[10];
    a[0] = ldnt(wp + w0);
    a[1] = ldnt(wp + w1);
    __builtin_amdgcn_sched_barrier(0);
    u32 t0 = (u32)__builtin_amdgcn_s_memrealtime(), far = t0 + 0x10000000u;
    asm volatile("" : "+s"(rw), "+s"(t0), "+s"(far));
    const GAS char *x = (const GAS char*)ka[0], *wo_ = (const GAS char*)ka[4], *wd_ = (const GAS char*)ka[7];
    GAS char* out_ = (GAS char*)kb[0];
    RSRC();
    const u32 b1 = rw + 2 + wave, b2 = rw + 6 + wave;
    const int row1 = (r & 1) * 320 + min(b1, b1 - 10) * 32 + g * 8, row2 = (r & 1) * 320 + min(b2, b2 - 10) * 32 + g * 8;
    const u32 xso = (lane >> 5) * 1280 + (lane & 31) * 40, xb0 = (i >> 3) * 1280;
    const v4u xa = *(const GAS v4u_a4*)(x + xso), xb = *(const GAS v4u_a4*)(x + xso + 16);
    const v2u xc = *(const GAS v2u_a4*)(x + xso + 32);
    const v2u xe = *(const GAS v2u_a4*)(x + (xb0 + row0 * 2));
    const v4u xd = *(const GAS v4u_a4*)(x + (xb0 + row1 * 2)), xf = *(const GAS v4u_a4*)(x + (xb0 + row2 * 2));
    const int ty = r >= 20 ? 2 : r >= 10 ? 1 : 0, cb = r - ty * 10;
    const u32 ep = __builtin_amdgcn_raw_buffer_load_b32(rs, tid >= 184 && tid < 192 ? line_off((cb * 4 + (lane & 3)) * 24 + xcd * 3 + ty) + (lane & 4) * 2048 : 0, 0, AUX_SC1);
    if (wave == 0 && lane < 9) *((volatile LAS u32*)(L + L_C) + lane) = lane < 8 ? 0u : far;
    w0 = col + (row1 + sl * 4) * stride;
    pace(t0 + (SC.first + wave * SC.d1) / 16);
#pragma unroll
    for (int t = 2; t < 6; ++t) { a[t] = ldnt(wp + w0); w0 += stride; }
    __builtin_amdgcn_sched_barrier(0);
    __builtin_amdgcn_s_barrier();
    CBAR();
    w0 = col + (row2 + sl * 4) * stride;
    pace(t0 + (SC.first + (wave + 4) * SC.d1) / 16);
#pragma unroll
    for (int t = 6; t < 10; ++t) { a[t] = ldnt(wp + w0); w0 += stride; }
    __builtin_amdgcn_sched_barrier(0);
    const u32 po = (lane & 16 ? 0 : XP_OFF + (lane & 8) * 32) + (lane & 7) * 16;
    const v4u z = {0u, 0u, 0u, 0u};
    if (wave == 1 && bid == 128)
      asm volatile("global_store_dwordx4 %0, %2, %3 sc1\n\tglobal_store_dwordx4 %1, %2, %4 sc1\n\ts_nop 1"
                   :: "v"(po), "v"(XP_OFF + (lane & 8) * 32 + (lane & 16) * 512 + (lane & 7) * 16), "v"(z), "s"(kb[0]), "s"(kb[1]) : "memory");
    u32 own = 0;
    if (wave == 0) {
      asm volatile("s_waitcnt vmcnt(8)" ::: "memory");
      own = t0 + max((int)((u32)__builtin_amdgcn_s_memrealtime() - t0) - SC.nom / 16, 0);
      asm volatile("" : "+s"(own));
      *(volatile LAS u32*)(L + V_ORG) = own;
      if (bid < 8 && lane == 0) __builtin_amdgcn_raw_buffer_store_b64(v2u{own, ep + 0x01010101u}, rs, XT_OFF + bid * 8, 0, AUX_SC1);
    }
    const u32 half = (i & 1) ? 0xFFFF0000u : 0x0000FFFFu;
    const int pos = (i & 7) >> 1;
    const u32 mk0 = pos == 0 ? half : 0u, mk1 = pos == 1 ? half : 0u, mk2 = pos == 2 ? half : 0u, mk3 = pos == 3 ? half : 0u;
    const u32 xs_[10] = {xa.x, xa.y, xa.z, xa.w, xb.x, xb.y, xb.z, xb.w, xc.x, xc.y};
    float s1 = 0.f;
#pragma unroll
    for (int k = 0; k < 10; ++k) s1 = dot2(xs_[k], 0x3F803F80u, s1);
    s1 = rowsum(s1);
    s1 = add16(s1, s1) * (1.f / 640.f);
    f32x2 m2 = {s1, s1}, q2 = {0.f, 0.f};
    asm volatile("" : "+v"(m2));
#pragma unroll
    for (int k = 0; k < 10; ++k) { const f32x2 d = f32x2{bflo(xs_[k]), bfhi(xs_[k])} - m2; q2 += d * d; }
    float s2 = q2[0], s3 = q2[1];
    asm volatile("" : "+v"(s2), "+v"(s3));
    s2 = rowsum(s2 + s3);
    s2 = add16(s2, s2) * (1.f / 640.f);
    auto p1 = __builtin_amdgcn_permlane32_swap(asu(s1), asu(s1), false, false);
    auto p2 = __builtin_amdgcn_permlane32_swap(asu(s2), asu(s2), false, false);
    const float mb = rbf(asf((i >> 3) ? p1[1] : p1[0]));
    const float rden = __builtin_amdgcn_rcpf(rbf(__builtin_amdgcn_sqrtf(rbf(rbf(asf((i >> 3) ? p2[1] : p2[0])) + 1e-5f))));
    f32x2 mb2 = {mb, mb};
    asm volatile("" : "+v"(mb2));
    const u32 tag = __builtin_amdgcn_readfirstlane(ep) + 0x01010101u;
    f32x4 acc0 = {0.f, 0.f, 0.f, 0.f}, acc1 = {0.f, 0.f, 0.f, 0.f};
#pragma unroll
    for (int t = 0; t < 10; ++t) {
      const u32 xw0 = t < 2 ? xe[0] : t < 6 ? xd[(t - 2) >> 1] : xf[(t - 6) >> 1], xw1 = t < 2 ? xe[1] : t < 6 ? xd[2 + ((t - 2) >> 1)] : xf[2 + ((t - 6) >> 1)];
      const f32x2 dx = f32x2{(t & 1) ? bfhi(xw0) : bflo(xw0), (t & 1) ? bfhi(xw1) : bflo(xw1)} - mb2;
      const u32 c = cvtpk(dx[0], dx[1]);
      const float n0 = bflo(c) * rden, n1 = bfhi(c) * rden;
      const u32 d0 = cvtpk(n0, n0), d1 = cvtpk(n1, n1);
      acc0 = mfma(a[t], v4u{d0 & mk0, d0 & mk1, d0 & mk2, d0 & mk3}, acc0);
      acc1 = mfma(a[t], v4u{d1 & mk0, d1 & mk1, d1 & mk2, d1 & mk3}, acc1);
    }
    asm volatile("s_nop 7\n\ts_nop 1" : "+v"(acc0), "+v"(acc1));
    const f32x4 zero = {0.f, 0.f, 0.f, 0.f};
    *(LAS f32x4*)(L + L_RED + wave * 1024 + lane * 16) = g < 2 ? (n == 8 ? zero : acc0) : (n == 3 ? zero : acc1);
    if (lds_arrive<C_P1>() == 3u) {
      const LAS f32x4* rp = (const LAS f32x4*)(L + L_RED + lane * 16);
      f32x4 s = (rp[0] + rp[64]) + (rp[128] + rp[192]);
#pragma unroll
      for (int k = 0; k < 4; ++k) s[k] = add32(s[k], s[k]);
      st_sc1(rs, lane < 32 ? X1_OFF + (xcd * 32 + r) * 512 + lane * 16 : WS_BYTES, pack_f(s, tag));
    }
    if (wave == 0) {
      pace(own + SC.sy / 16);
      const v2u st = __builtin_amdgcn_raw_buffer_load_b64(rs, XT_OFF + (lane & 7) * 8, 0, AUX_SC1);
      const int dt = (int)(st.x - own);
      const bool ok = st.y == tag && dt > -400 && dt < 400;
      float sd = ok ? (float)dt : 0.f, sn = ok ? 1.f : 0.f, mx = ok ? (float)dt : -400.f;
      sd += dppf<XOR1>(sd); sd += dppf<XOR2>(sd); sd += dppf<XOR7>(sd);
      sn += dppf<XOR1>(sn); sn += dppf<XOR2>(sn); sn += dppf<XOR7>(sn);
      mx = fmaxf(mx, dppf<XOR1>(mx)); mx = fmaxf(mx, dppf<XOR2>(mx)); mx = fmaxf(mx, dppf<XOR7>(mx));
      sd *= __builtin_amdgcn_rcpf(fmaxf(sn, 1.f));
      mx = fmaxf(mx, sd);
      *(volatile LAS u32*)(L + V_ORG) = own + (int)(sd + (mx - sd) * ((float)SC.alpha * 0.0625f));
      const int u = lane >> 5, cq = (lane >> 4) & 1, gq = (lane >> 3) & 1, c7 = lane & 7;
      f32x4 v[2];
      pace(own + SC.qg / 16);
      pace_org(SC.qg / 16);
      const bool shared = (r >> 3) == 1 && cq;
      gather_f<2>(rs, X1_OFF + (xcd * 32 + (r >> 3) * 4 + cq * 2 + u) * 512 + (gq * 16 + (j >> 2) * 8 + c7) * 16, shared ? 10 * 512 : 0, tag, v);
      const f32x4 s = v[0] + (shared ? v[1] : zero);
      const u32 pk = cvtpk(add32(s[0], s[2]), add32(s[1], s[3]));
      LAS unsigned short* qd = (LAS unsigned short*)(L + L_Q) + cq * 64 + gq * 32 + u * 16 + c7;
      qd[0] = (unsigned short)pk;
      qd[8] = (unsigned short)(pk >> 16);
      *(volatile LAS u32*)(L + V_TAG) = tag;
      lds_signal<F_Q>();
    } else if (wave >= 2 && bid >= 240 && bid * 2 + wave < 482 + 27) {
      const int fb = bid * 2 + wave - 482, u = lane & 1;
      const int og = (fb * 2 / 7 * 18 + fb * 4) * 512, ou = ((fb * 2 + 1) / 7 * 18 + fb * 4 + 2) * 512;
      f32x4 v[2];
      pace_org(SC.hg / 16);
      gather_f<2>(rs, X1_OFF + 18 * 512 + og + u * 512 + ((lane & 2) * 8 + (lane >> 2)) * 16, ou - og, tag, v);
      const f32x4 gs = v[0], us = v[1];
      const float g0 = (u ? gs[2] : gs[0]) + dppf<XOR1>(u ? gs[0] : gs[2]), g1 = (u ? gs[3] : gs[1]) + dppf<XOR1>(u ? gs[1] : gs[3]);
      const float u0 = (u ? us[2] : us[0]) + dppf<XOR1>(u ? us[0] : us[2]), u1 = (u ? us[3] : us[1]) + dppf<XOR1>(u ? us[1] : us[3]);
      const float h0 = rbf(gelu_tanh(rbf(g0))) * rbf(u0), h1 = rbf(gelu_tanh(rbf(g1))) * rbf(u1);
      __builtin_amdgcn_raw_buffer_store_b64(v2u{cvtpk(h0, h1), u ? 0x3F800000u | ((tag >> 16) & 0xFFu) : tag & 0xFFFFu}, rs,
                                            XH_OFF + (lane >> 5) * 8192 + fb * 256 + (lane & 31) * 8, 0, AUX_SC1);
    }
    if (wave == 1 && bid == 128) {
      asm volatile("s_waitcnt vmcnt(0)" ::: "memory");
      u32 la = ~0u, lb = ~0u, lc = ~0u;
#pragma nounroll
      for (u32 k = XP_OFF; k <= XP_OFF + 256; k += 256) {
        u64 c0, c1, c2, c3; u32 d;
        asm volatile("s_memtime %0\n\ts_waitcnt lgkmcnt(0)\n\ts_load_dword %4, %5, %8\n\ts_waitcnt lgkmcnt(0)\n\t"
                     "s_memtime %1\n\ts_waitcnt lgkmcnt(0)\n\ts_load_dword %4, %6, %8\n\ts_waitcnt lgkmcnt(0)\n\t"
                     "s_memtime %2\n\ts_waitcnt lgkmcnt(0)\n\ts_load_dword %4, %7, %8\n\ts_waitcnt lgkmcnt(0)\n\t"
                     "s_memtime %3\n\ts_waitcnt lgkmcnt(0)"
                     : "=&s"(c0), "=&s"(c1), "=&s"(c2), "=&s"(c3), "=&s"(d) : "s"(kb[1]), "s"(kb[1] + 8192), "s"(kb[0]), "s"(k) : "memory");
        la = min(la, (u32)c1 - (u32)c0); lb = min(lb, (u32)c2 - (u32)c1); lc = min(lc, (u32)c3 - (u32)c2);
      }
      asm volatile("global_store_dwordx4 %0, %1, %2 sc1\n\ts_waitcnt vmcnt(0)" :: "v"(po), "v"(z), "s"(kb[0]) : "memory");
      const u32 away = 2 * lc > la + lb;
      __builtin_amdgcn_raw_buffer_store_b64(v2u{(tag & ~0x2001u) | away | (((lb < la) ^ away) << 13), tag}, rs, XS_OFF, 0, AUX_SC1);
    }
    if (wave == 1 && bid == 255) {
      f32x4 v[4];
      pace_org(SC.sw2 / 16);
#pragma nounroll
      for (int p = 0; p < 64; p += 16) gather_f<4>(rs, X1_OFF + lane * 512 + p, 32768, tag, v);
      __builtin_amdgcn_raw_buffer_store_b32(tag, rs, 0, 0, AUX_SC1);
    }
    const int fu = r * 4 + (xcd & 3);
    bool fin = (wave == 1 || wave == 2) && r < 5;
    u32 ob = ((fu & 1) * 640 + (fu >> 1) * 64 + (((wave - 1) & 1) + 2 * (g & 1)) * 16 + (lane & 7) * 2 + (g >> 1)) * 2;
    const int l3 = (fu * 2 + wave - 1) * 24;
    int x3i = line_off(l3) + lane * 16;
    float xv = 0.f;
    if (fin) {
      xv = bflo(*(const GAS unsigned short*)(x + ob));
      u32 d;
      asm volatile("s_load_dword %0, %1, 0x0\n\ts_waitcnt lgkmcnt(0)" : "=&s"(d) : "s"(out_) : "memory");
    }
    if (r < 30) {
      const int xs = xcd ^ 1;
      const bool live = ty < 2 || wave < 3 || xcd < 3;
      const int blk = ty < 2 ? xs * 8 + ty * 4 + wave : xcd * 3 + min(xcd, 3) + (live ? wave : 0);
      const GAS char* src = (ty < 2 ? wo_ : wd_) + (u32)blk * (64 * 1280) + cb * 128;
      const int lr = lane >> 3, wr = (lr & 4) | ((lr & 1) << 1) | ((lr >> 1) & 1);
      u32 vo[4];
#pragma unroll
      for (int q = 0; q < 4; ++q) vo[q] = (8 * q + wr) * 1280 + (((lane & 7) ^ (2 * q)) * 16);
      int o0 = (ty < 2 ? X2_OFF + (blk >> 1) * 4096 + (i >> 2) * 1024 + (blk & 1) * 256 : XH_OFF + (i >> 3) * 8192 + blk * 256) + g * 64 + (i & 3) * 16;
      u32 so = ty < 2 ? 512 : 0;
      const int q4 = (lane >> 2) & 3;
      u32 wb[4];
#pragma unroll
      for (int t = 0; t < 4; ++t) wb[t] = L_W + wave * 8192 + q4 * 1024 + ((g >> 1) * 4 + (g & 1)) * 128 + (lane & 3) * 8 + (t ^ q4) * 32;
      if (!live) {
#pragma unroll
        for (int q = 0; q < 8; ++q) *(LAS v4u*)(L + L_W + 3 * 8192 + q * 1024 + lane * 16) = v4u{0u, 0u, 0u, 0u};
      }
      const u64 lm = live ? ~0ull : 1ull;
      const u32 tw2 = (SC.sw2 + ((wave + (((u32)(ty * 8 + xcd) * 2654435761u) >> 30)) & 3) * SC.d2) / 16;
      const u32 dst = live ? L_W + wave * 8192 : L_SPARE;
      int x3o = lane < 32 ? line_off((cb * 4 + (lane >> 3)) * 24 + xcd * 3 + ty) + (lane & 7) * 16 : WS_BYTES;
      u32 yw = L_Y + wave * 512 + lane * 8, yr = L_Y + (lane & 31) * 16;
      v4u va, vb, bA[4];
      u64 lo = 0x3333333333333333ull;
      u32 cm = 0xFFFF0000u, cl = 0xFFFFFF00u, tg2 = tag >> 16, cnt = SPIN_CAP;
      float ce = LOG2E;
      u32 n0 = tag >> 8, n1 = tag >> 24;
      asm volatile("s_waitcnt vmcnt(0) lgkmcnt(0)" : "+v"(x3o), "+v"(yw), "+v"(yr), "+v"(wb[0]), "+v"(wb[1]), "+v"(wb[2]), "+v"(wb[3]), "+v"(ob), "+v"(x3i), "+v"(xv),
                   "+v"(o0), "+v"(cm), "+v"(cl), "+v"(ce), "+v"(n0), "+v"(n1), "+s"(lo), "+s"(tg2), "+s"(cnt), "+s"(so) :: "memory");
      pace_org(tw2);
      v2u sd;
      asm volatile("buffer_load_dwordx2 %0, off, %1, 0 offset:%2 sc1" : "=&{v[62:63]}"(sd) : "s"(rs), "i"(XS_OFF) : "memory");
      DMA4(src, dst, lm);
      pace_org(tw2 + SC.d2 / 4);
      DMA4(src + 32 * 1280, dst + 4096, lm);
      pace_org(SC.x2g / 16);
      asm volatile("buffer_load_dwordx4 %0, %2, %3, 0 offen sc1\n\tbuffer_load_dwordx4 %1, %2, %3, %4 offen sc1\n\ts_waitcnt vmcnt(6)"
                   : "=&{v[64:67]}"(va), "=&{v[68:71]}"(vb) : "v"(o0), "s"(rs), "s"(so) : "memory");
#pragma unroll
      for (int t = 0; t < 4; ++t) bA[t] = trfrag((LAS char*)(size_t)wb[t]);
      {
        u32 sa, sb, left = SPIN_CAP;
        asm volatile("1:\n\tv_readfirstlane_b32 %0, v62\n\tv_readfirstlane_b32 %1, v63\n\t"
                     "s_xor_b32 %1, %1, %4\n\ts_xor_b32 s90, %0, %4\n\ts_andn2_b32 s90, s90, 0x2001\n\ts_or_b32 %1, %1, s90\n\ts_cbranch_scc0 2f\n\t"
                     "s_sub_u32 %2, %2, 1\n\ts_cbranch_scc1 2f\n\t"
                     "buffer_load_dwordx2 %3, off, %5, 0 offset:%6 sc1\n\ts_waitcnt vmcnt(0)\n\ts_branch 1b\n2:"
                     : "=&s"(sa), "=&s"(sb), "+s"(left), "+{v[62:63]}"(sd) : "s"(tag), "s"(rs), "i"(XS_OFF) : "memory", "scc", "s90");
        sa = __builtin_amdgcn_readfirstlane(sa);
        x3o += sa & 0x2000u; x3i += sa & 0x2000u;
        fin = fin && (sa & 1u) == (u32)(xcd >> 2);
        asm volatile("" : "+v"(x3o), "+v"(x3i));
      }
      asm volatile(
          "s_branch 4f\n"
          "3:\n\t"
          "s_sub_u32 %[cnt], %[cnt], 1\n\t"
          "s_cbranch_scc1 2f\n\t"
          "buffer_load_dwordx4 v[64:67], %[o0], %[rs], 0 offen sc1\n\tbuffer_load_dwordx4 v[68:71], %[o0], %[rs], %[so] offen sc1\n\t"
          "s_waitcnt vmcnt(0)\n\t"
          "v_bitop3_b32 v72, v65, v69, %[tg] bitop3:0x7e\n\tv_bitop3_b32 v73, v67, v71, %[tg2] bitop3:0x7e\n\t"
          "v_lshlrev_b32 v73, 24, v73\n\tv_lshl_or_b32 v72, v72, 16, v73\n\tv_cmp_ne_u32 vcc, 0, v72\n\t"
          "s_cbranch_vccnz 3b\n\ts_branch 1f\n"
          "4:\n\t"
          "s_waitcnt vmcnt(2)\n\t"
          "ds_read_b64_tr_b16 v[96:97], %[w0] offset:4096\n\tds_read_b64_tr_b16 v[98:99], %[w0] offset:4352\n\t"
          "ds_read_b64_tr_b16 v[100:101], %[w1] offset:4096\n\tds_read_b64_tr_b16 v[102:103], %[w1] offset:4352\n\t"
          "ds_read_b64_tr_b16 v[104:105], %[w2] offset:4096\n\tds_read_b64_tr_b16 v[106:107], %[w2] offset:4352\n\t"
          "ds_read_b64_tr_b16 v[108:109], %[w3] offset:4096\n\tds_read_b64_tr_b16 v[110:111], %[w3] offset:4352\n"
          "1:\n\t"
          "s_mov_b64 vcc, %[lo]\n\t"
          "s_waitcnt vmcnt(0)\n\t"
          "v_cndmask_b32_dpp v72, v68, v64, vcc quad_perm:[2,3,0,1] row_mask:0xf bank_mask:0xf\n\t"
          "v_cndmask_b32_dpp v73, v70, v66, vcc quad_perm:[2,3,0,1] row_mask:0xf bank_mask:0xf\n\t"
          "v_cndmask_b32 v112, v69, v65, vcc\n\t"
          "v_cndmask_b32 v114, v71, v67, vcc\n\t"
          "s_not_b64 vcc, vcc\n\t"
          "v_cndmask_b32_dpp v74, v64, v68, vcc quad_perm:[2,3,0,1] row_mask:0xf bank_mask:0xf\n\t"
          "v_cndmask_b32_dpp v75, v66, v70, vcc quad_perm:[2,3,0,1] row_mask:0xf bank_mask:0xf\n\t"
          "v_and_b32 v112, %[cm], v112\n\t"
          "v_and_b32 v114, %[cl], v114\n\t"
          "v_mfma_f32_16x16x32_bf16 v[80:83], v[72:75], %[A0], 0\n\t"
          "v_mov_b32_dpp v76, v72 quad_perm:[1,0,3,2] row_mask:0xf bank_mask:0xf\n\t"
          "v_max_f32_dpp v113, v112, v112 quad_perm:[2,3,0,1] row_mask:0xf bank_mask:0xf\n\t"
          "v_mfma_f32_16x16x32_bf16 v[84:87], v[72:75], %[A1], 0\n\t"
          "v_mov_b32_dpp v77, v73 quad_perm:[1,0,3,2] row_mask:0xf bank_mask:0xf\n\t"
          "v_mov_b32_dpp v78, v74 quad_perm:[1,0,3,2] row_mask:0xf bank_mask:0xf\n\t"
          "v_mfma_f32_16x16x32_bf16 v[88:91], v[72:75], %[A2], 0\n\t"
          "v_max_f32_dpp v113, v113, v113 row_half_mirror row_mask:0xf bank_mask:0xf\n\t"
          "v_mov_b32_dpp v79, v75 quad_perm:[1,0,3,2] row_mask:0xf bank_mask:0xf\n\t"
          "v_mfma_f32_16x16x32_bf16 v[92:95], v[72:75], %[A3], 0\n\t"
          "v_sub_f32 v112, v112, v113\n\t"
          "v_mul_f32 v112, %[ce], v112\n\t"
          "s_waitcnt lgkmcnt(0)\n\t"
          "v_mfma_f32_16x16x32_bf16 v[80:83], v[76:79], v[96:99], v[80:83]\n\t"
          "v_exp_f32 v112, v112\n\t"
          "v_bitop3_b32 v65, v65, v69, %[tg] bitop3:0x7e\n\t"
          "v_mfma_f32_16x16x32_bf16 v[84:87], v[76:79], v[100:103], v[84:87]\n\t"
          "v_mul_f32 v113, v112, v114\n\t"
          "v_bitop3_b32 v67, v67, v71, %[tg2] bitop3:0x7e\n\t"
          "v_mfma_f32_16x16x32_bf16 v[88:91], v[76:79], v[104:107], v[88:91]\n\t"
          "v_lshlrev_b32 v67, 24, v67\n\t"
          "v_add_f32_dpp v113, v113, v113 quad_perm:[2,3,0,1] row_mask:0xf bank_mask:0xf\n\t"
          "v_mfma_f32_16x16x32_bf16 v[92:95], v[76:79], v[108:111], v[92:95]\n\t"
          "v_lshl_or_b32 v65, v65, 16, v67\n\t"
          "v_cmp_ne_u32 vcc, 0, v65\n\t"
          "v_add_f32_dpp v113, v113, v113 row_half_mirror row_mask:0xf bank_mask:0xf\n\t"
          "v_mov_b32_dpp v112, v112 row_shl:4 row_mask:0xa bank_mask:0xf\n\t"
          "v_rcp_f32 v113, v113\n\t"
          "s_nop 0\n\t"
          "v_mov_b32_dpp v112, v112 row_ror:8 row_mask:0xc bank_mask:0xf\n\t"
          "s_cbranch_vccnz 3b\n"
          "2:\n\t"
          "v_mov_b32_dpp v113, v113 row_ror:8 row_mask:0xc bank_mask:0xf\n\t"
          "v_mul_f32_dpp v80, v112, v80 row_newbcast:0 row_mask:0xf bank_mask:0xf\n\t"
          "v_mul_f32_dpp v84, v112, v84 row_newbcast:0 row_mask:0xf bank_mask:0xf\n\t"
          "v_mul_f32_dpp v88, v112, v88 row_newbcast:0 row_mask:0xf bank_mask:0xf\n\t"
          "v_mul_f32_dpp v92, v112, v92 row_newbcast:0 row_mask:0xf bank_mask:0xf\n\t"
          "v_fmac_f32_dpp v80, v112, v82 row_newbcast:2 row_mask:0xf bank_mask:0xf\n\t"
          "v_fmac_f32_dpp v84, v112, v86 row_newbcast:2 row_mask:0xf bank_mask:0xf\n\t"
          "v_fmac_f32_dpp v88, v112, v90 row_newbcast:2 row_mask:0xf bank_mask:0xf\n\t"
          "v_fmac_f32_dpp v92, v112, v94 row_newbcast:2 row_mask:0xf bank_mask:0xf\n\t"
          "v_permlane16_swap_b32 v80, v84\n\t"
          "s_nop 0\n\t"
          "v_permlane16_swap_b32 v88, v92\n\t"
          "v_add_f32 v80, v80, v84\n\t"
          "v_add_f32 v88, v88, v92\n\t"
          "s_nop 0\n\t"
          "v_mul_f32_dpp v72, v113, v80 row_newbcast:0 row_mask:0xf bank_mask:0xf\n\t"
          "v_mul_f32_dpp v73, v113, v88 row_newbcast:0 row_mask:0xf bank_mask:0xf\n\t"
          "ds_write_b64 %[yw], v[72:73]\n\t"
          "s_cmp_lg_u32 %[wv], 0\n\t"
          "s_waitcnt lgkmcnt(0)\n\t"
          "s_barrier\n\t"
          "s_cbranch_scc1 5f\n\t"
          "ds_read_b128 v[72:75], %[yr]\n\tds_read_b128 v[76:79], %[yr] offset:512\n\tds_read_b128 v[80:83], %[yr] offset:1024\n\tds_read_b128 v[84:87], %[yr] offset:1536\n\t"
          "s_waitcnt lgkmcnt(2)\n\tv_pk_add_f32 v[72:73], v[72:73], v[76:77]\n\tv_pk_add_f32 v[74:75], v[74:75], v[78:79]\n\t"
          "s_waitcnt lgkmcnt(0)\n\tv_pk_add_f32 v[80:81], v[80:81], v[84:85]\n\tv_pk_add_f32 v[82:83], v[82:83], v[86:87]\n\t"
          "v_pk_add_f32 v[72:73], v[72:73], v[80:81]\n\tv_pk_add_f32 v[74:75], v[74:75], v[82:83]\n\t"
          "v_bfi_b32 v72, %[cl], v72, %[tg]\n\tv_bfi_b32 v73, %[cl], v73, %[n0]\n\tv_bfi_b32 v74, %[cl], v74, %[tg2]\n\tv_bfi_b32 v75, %[cl], v75, %[n1]\n\t"
          "buffer_store_dwordx4 v[72:75], %[x3o], %[rs], 0 offen sc1\n\ts_nop 1\n"
          "5:"
          : "+{v[64:67]}"(va), "+{v[68:71]}"(vb), [cnt] "+s"(cnt)
          : [w0] "v"(wb[0]), [w1] "v"(wb[1]), [w2] "v"(wb[2]), [w3] "v"(wb[3]), [A0] "v"(bA[0]), [A1] "v"(bA[1]), [A2] "v"(bA[2]), [A3] "v"(bA[3]),
            [cm] "v"(cm), [cl] "v"(cl), [ce] "v"(ce), [o0] "v"(o0), [lo] "s"(lo), [tg] "s"(tag), [tg2] "s"(tg2), [rs] "s"(rs), [so] "s"(so),
            [yw] "v"(yw), [yr] "v"(yr), [x3o] "v"(x3o), [n0] "v"(n0), [n1] "v"(n1), [wv] "s"(wave)
          : "memory", "vcc", "scc", "v72", "v73", "v74", "v75", "v76", "v77", "v78", "v79", "v80", "v81", "v82", "v83", "v84", "v85", "v86", "v87", "v88", "v89", "v90", "v91",
            "v92", "v93", "v94", "v95", "v96", "v97", "v98", "v99", "v100", "v101", "v102", "v103", "v104", "v105", "v106", "v107", "v108", "v109", "v110", "v111", "v112", "v113", "v114");
    }
    if (fin) {
      u32 n0 = tag & 255u, n1 = (tag >> 8) & 255u, n2 = (tag >> 16) & 255u, n3 = tag >> 24, ff = 255u, cnt = SPIN_CAP;
      u32 s1k = line_off(l3 + 8) - line_off(l3), s2k = line_off(l3 + 16) - line_off(l3);
      asm volatile("" : "+s"(n0), "+s"(n1), "+s"(n2), "+s"(n3), "+s"(ff), "+s"(cnt), "+s"(s1k), "+s"(s2k));
      pace((u32)__builtin_amdgcn_s_memrealtime() + SC.x3s / 16);
      pace_org(SC.x3g / 16);
      asm volatile(
          "1:\n\t"
          "buffer_load_dwordx4 v[64:67], %[o], %[rs], 0 offen sc1\n\tbuffer_load_dwordx4 v[68:71], %[o], %[rs], %[s1] offen sc1\n\t"
          "buffer_load_dwordx4 v[72:75], %[o], %[rs], %[s2] offen sc1\n\t"
          "s_sub_u32 %[cnt], %[cnt], 1\n\t"
          "s_waitcnt vmcnt(2)\n\t"
          "v_xor_b32 v64, %[n0], v64\n\tv_xor_b32 v65, %[n1], v65\n\tv_xor_b32 v66, %[n2], v66\n\tv_xor_b32 v67, %[n3], v67\n\t"
          "v_or3_b32 v76, v64, v65, v66\n\t"
          "s_waitcnt vmcnt(1)\n\t"
          "v_xor_b32 v68, %[n0], v68\n\tv_xor_b32 v69, %[n1], v69\n\tv_xor_b32 v70, %[n2], v70\n\tv_xor_b32 v71, %[n3], v71\n\t"
          "v_or3_b32 v76, v76, v67, v68\n\tv_or3_b32 v77, v69, v70, v71\n\t"
          "v_pk_add_f32 v[64:65], v[64:65], v[68:69]\n\tv_pk_add_f32 v[66:67], v[66:67], v[70:71]\n\t"
          "s_waitcnt vmcnt(0)\n\t"
          "v_xor_b32 v72, %[n0], v72\n\tv_xor_b32 v73, %[n1], v73\n\tv_xor_b32 v74, %[n2], v74\n\tv_xor_b32 v75, %[n3], v75\n\t"
          "v_pk_add_f32 v[64:65], v[64:65], v[72:73]\n\tv_pk_add_f32 v[66:67], v[66:67], v[74:75]\n\t"
          "v_or3_b32 v76, v76, v72, v73\n\tv_or3_b32 v77, v77, v74, v75\n\t"
          "v_permlane32_swap_b32 v64, v66\n\tv_permlane32_swap_b32 v65, v67\n\t"
          "v_add_f32 v64, v64, v66\n\tv_add_f32 v65, v65, v67\n\t"
          "v_bitop3_b32 v76, v76, %[ff], v77 bitop3:0xc8\n\tv_cmp_ne_u32 vcc, 0, v76\n\t"
          "v_permlane16_swap_b32 v64, v65\n\t"
          "v_add_f32 v64, v64, v65\n\t"
          "s_cbranch_scc1 2f\n\t"
          "s_nop 0\n\t"
          "v_add_f32_dpp v64, v64, v64 row_ror:8 row_mask:0xf bank_mask:0xf\n\t"
          "v_add_f32 v64, v64, %[xv]\n\t"
          "v_cvt_pk_bf16_f32 v64, v64, v64\n\t"
          "s_cbranch_vccnz 1b\n\t"
          "global_store_short %[ob], v64, %[out]\n"
          "2:"
          : [cnt] "+s"(cnt)
          : [o] "v"(x3i), [rs] "s"(rs), [s1] "s"(s1k), [s2] "s"(s2k), [n0] "s"(n0), [n1] "s"(n1), [n2] "s"(n2), [n3] "s"(n3), [ff] "s"(ff), [xv] "v"(xv), [ob] "v"(ob), [out] "s"(out_)
          : "memory", "vcc", "scc", "v64", "v65", "v66", "v67", "v68", "v69", "v70", "v71", "v72", "v73", "v74", "v75", "v76", "v77");
    }
  } else {
    __builtin_amdgcn_s_barrier();
    CBAR();
    int tid = threadIdx.x, bid = blockIdx.x;
    asm volatile("" : "+v"(tid));
    asm volatile("" : "+s"(bid));
    IDS();
    RSRC();
    const GAS char *kc_ = (const GAS char*)ka[1], *vc_ = (const GAS char*)ka[2];
    const int aw = wave - 4;
    const size_t kvo = ((size_t)((j >> 2) * 32 + h) * 4096 + 3072 + (j & 3) * 256 + (aw & 3) * 16) * 256;
    const GAS char* kp = kc_ + kvo;
    const GAS char* vp = vc_ + kvo;
    const u32 rot = (((u32)bid * 2654435761u) >> 30) + (aw >> 2);
    const u32 bo0 = (rot & 3) * 16384 + lane * 16, bo1 = bo0 ^ 32768;
    const u32 tw = (SC.sk + aw * SC.dk) / 16, tv = (SC.sv + aw * SC.dv) / 16, tl = tv + SC.dv / 2;
    v4u kk[8], vv[8];
#define LD4(d, n, off, base) asm volatile("global_load_dwordx4 %0, %4, %5 nt\n\tglobal_load_dwordx4 %1, %4, %5 offset:1024 nt\n\t" \
      "global_load_dwordx4 %2, %4, %5 offset:2048 nt\n\tglobal_load_dwordx4 %3, %4, %5 offset:3072 nt" \
      : "=&v"(d[n]), "=&v"(d[n + 1]), "=&v"(d[n + 2]), "=&v"(d[n + 3]) : "v"(off), "s"(base) : "memory")
#define LAST(when) do { u32 t_, z_ = 0; asm volatile("s_cmp_lg_u32 %8, " #when "\n\ts_cbranch_scc1 2f\n" \
      "1:\n\tds_read_b32 %4, %5 offset:%10\n\ts_memrealtime s[90:91]\n\ts_waitcnt lgkmcnt(0)\n\tv_sub_u32 %4, s90, %4\n\tv_cmp_gt_i32 vcc, %9, %4\n\ts_cbranch_vccnz 1b\n\t" \
      "global_load_dwordx4 %0, %6, %7 nt\n\tglobal_load_dwordx4 %1, %6, %7 offset:1024 nt\n\t" \
      "global_load_dwordx4 %2, %6, %7 offset:2048 nt\n\tglobal_load_dwordx4 %3, %6, %7 offset:3072 nt\n2:" \
      : "+v"(vv[4]), "+v"(vv[5]), "+v"(vv[6]), "+v"(vv[7]), "=&v"(t_) : "v"(z_), "v"(bo1), "s"(vp), "s"(early), "s"(tl), "i"(V_ORG) \
      : "memory", "vcc", "scc", "s90", "s91"); } while (0)
#define WAITN(n) "s_waitcnt vmcnt(" #n " + 4)\n\ts_cmp_eq_u32 %[e], 0\n\ts_cbranch_scc1 1f\n\ts_waitcnt vmcnt(" #n ")\n1:"
    pace_org(tw); LD4(kk, 0, bo0, kp);
    pace_org(tw + SC.dk / 2); LD4(kk, 4, bo1, kp);
    pace_org(tv); LD4(vv, 0, bo0, vp);
    const u32 early = flag_or_time<F_Q>(tl - 50);
    asm volatile("" : "=v"(vv[4]), "=v"(vv[5]), "=v"(vv[6]), "=v"(vv[7]));
    LAST(0);
    lds_wait<F_Q, 1>();
    asm volatile(WAITN(4) : "+v"(kk[0]), "+v"(kk[1]), "+v"(kk[2]), "+v"(kk[3]), "+v"(kk[4]), "+v"(kk[5]), "+v"(kk[6]), "+v"(kk[7]) : [e] "s"(early) : "memory", "scc");
    const u32 half = (i & 1) ? 0xFFFF0000u : 0x0000FFFFu;
    const int pos = (i & 7) >> 1;
    const u32 mk0 = pos == 0 ? half : 0u, mk1 = pos == 1 ? half : 0u, mk2 = pos == 2 ? half : 0u, mk3 = pos == 3 ? half : 0u;
    const v4u q = *(LAS v4u*)(L + L_Q + i * 16);
    const u32 tag = __builtin_amdgcn_readfirstlane(*(volatile LAS u32*)(L + V_TAG));
    float sx[8];
#pragma unroll
    for (int t = 0; t < 8; ++t) sx[t] = dot2(kk[t].w, q.w, dot2(kk[t].z, q.z, dot2(kk[t].y, q.y, dot2(kk[t].x, q.x, 0.f))));
#define TR_STEP(HALF, CTRL, BIT) _Pragma("unroll") for (int n = 0; n < (HALF); ++n) { \
      const float lo_ = sx[n], hi_ = sx[n + (HALF)]; const float keep_ = (i & (BIT)) ? hi_ : lo_, send_ = (i & (BIT)) ? lo_ : hi_; \
      sx[n] = keep_ + dppf<CTRL>(send_); }
    TR_STEP(4, XOR7, 4) TR_STEP(2, XOR2, 2) TR_STEP(1, XOR1, 1)
#undef TR_STEP
    float sc = rbf(rbf(sx[0] + dppf<ROR8>(sx[0])) * 0.08838834764831845f);
    float m = fmaxf(sc, dppf<XOR1>(sc)); m = fmaxf(m, dppf<XOR2>(m)); m = fmaxf(m, dppf<XOR7>(m)); m = max32(max16(m));
    const float e = __builtin_amdgcn_exp2f((sc - m) * LOG2E);
    float l = e + dppf<XOR1>(e); l += dppf<XOR2>(l); l += dppf<XOR7>(l); l = add16(l, l); l = add32(l, l);
    u32 pd = cvtpk(e, e);
    *(LAS f32x2*)(L + L_ML + aw * 8) = f32x2{m, l};
    lds_signal<C_ML>();
    __builtin_amdgcn_sched_barrier(0);
    LAST(1);
    f32x4 acc = {0.f, 0.f, 0.f, 0.f};
#define DIAGB(t) v4u{dppu<BCAST + t>(pd) & mk0, dppu<BCAST + t>(pd) & mk1, dppu<BCAST + t>(pd) & mk2, dppu<BCAST + t>(pd) & mk3}
    asm volatile("s_waitcnt vmcnt(4)" : "+v"(vv[0]), "+v"(vv[1]), "+v"(vv[2]), "+v"(vv[3]) :: "memory");
    acc = mfma(vv[0], DIAGB(0), acc); acc = mfma(vv[1], DIAGB(1), acc); acc = mfma(vv[2], DIAGB(2), acc); acc = mfma(vv[3], DIAGB(3), acc);
    v4u p4 = DIAGB(4), p5 = DIAGB(5), p6 = DIAGB(6), p7 = DIAGB(7);
#undef DIAGB
    asm volatile("" : "+v"(acc), "+v"(p4), "+v"(p5), "+v"(p6), "+v"(p7));
    lds_wait<C_ML, 8>();
    const f32x2 ml = *(LAS f32x2*)(L + L_ML + (lane & 7) * 8);
    float M = fmaxf(ml[0], dppf<XOR1>(ml[0])); M = fmaxf(M, dppf<XOR2>(M)); M = fmaxf(M, dppf<XOR7>(M));
    float Ls = __builtin_amdgcn_exp2f((ml[0] - M) * LOG2E) * ml[1]; Ls += dppf<XOR1>(Ls); Ls += dppf<XOR2>(Ls); Ls += dppf<XOR7>(Ls);
    float f = __builtin_amdgcn_exp2f((m - M) * LOG2E);
    u32 tm = asu(M) | (tag & 0xFFFFu), tq = f24(Ls, (tag >> 16) & 0xFFu);
    u32 oa = L_O + (g * 8 + (i & 7)) * 16;
    int x2o = i < 8 ? X2_OFF + h * 4096 + j * 512 + (g >> 1) * 256 + i * 32 + (g & 1) * 16 : WS_BYTES;
    u32 sig = aw < 5 ? C_O : C_O2, one = 1;
    asm volatile("" : "+v"(f), "+v"(tm), "+v"(tq), "+v"(x2o), "+v"(sig), "+v"(one));
    f32x4 s = {0.f, 0.f, 0.f, 0.f};
    if (aw == 7) {
      lds_wait<C_O, 5>();
      const LAS f32x4* op = (const LAS f32x4*)(size_t)oa;
      s = ((op[0] + op[32]) + (op[64] + op[96])) + op[128];
    } else {
      oa += aw * 512;
    }
    v4u pc = {0u, tm, 0u, tq};
    asm volatile("s_cmp_lg_u32 %[aw], 7\n\t"
                 "s_waitcnt vmcnt(3)\n\tv_mfma_f32_16x16x32_bf16 v[80:83], %[v4], %[p4], v[80:83]\n\ts_waitcnt vmcnt(2)\n\tv_mfma_f32_16x16x32_bf16 v[80:83], %[v5], %[p5], v[80:83]\n\t"
                 "s_waitcnt vmcnt(1)\n\tv_mfma_f32_16x16x32_bf16 v[80:83], %[v6], %[p6], v[80:83]\n\ts_waitcnt vmcnt(0)\n\tv_mfma_f32_16x16x32_bf16 v[80:83], %[v7], %[p7], v[80:83]\n\t"
                 "s_cbranch_scc1 1f\n\t"
                 "ds_read_b32 v100, %[sig]\n\tds_read_b128 v[92:95], %[oa] offset:2560\n\tds_read_b128 v[96:99], %[oa] offset:3072\n\t"
                 "s_nop 4\n\t"
                 "v_fmac_f32 v84, v80, %[f]\n\tv_fmac_f32 v85, v81, %[f]\n\tv_fmac_f32 v86, v82, %[f]\n\tv_fmac_f32 v87, v83, %[f]\n"
                 "3:\n\ts_waitcnt lgkmcnt(0)\n\t"
                 "v_cmp_ne_u32 vcc, 2, v100\n\t"
                 "v_pk_add_f32 v[92:93], v[92:93], v[96:97]\n\tv_pk_add_f32 v[94:95], v[94:95], v[98:99]\n\t"
                 "v_pk_add_f32 v[92:93], v[92:93], v[84:85]\n\tv_pk_add_f32 v[94:95], v[94:95], v[86:87]\n\t"
                 "v_cvt_pk_bf16_f32 v88, v92, v93\n\tv_cvt_pk_bf16_f32 v90, v94, v95\n\t"
                 "s_cbranch_vccnz 4f\n\t"
                 "buffer_store_dwordx4 v[88:91], %[xo], %[rs], 0 offen sc1\n\ts_branch 2f\n"
                 "4:\n\tds_read_b32 v100, %[sig]\n\tds_read_b128 v[92:95], %[oa] offset:2560\n\tds_read_b128 v[96:99], %[oa] offset:3072\n\ts_branch 3b\n"
                 "1:\n\ts_nop 7\n\t"
                 "v_mul_f32 v84, v80, %[f]\n\tv_mul_f32 v85, v81, %[f]\n\tv_mul_f32 v86, v82, %[f]\n\tv_mul_f32 v87, v83, %[f]\n\t"
                 "ds_write_b128 %[oa], v[84:87]\n\ts_mov_b64 exec, 1\n\tds_add_u32 %[sig], %[one]\n\ts_mov_b64 exec, -1\n"
                 "2:\n\ts_nop 0"
                 : "+{v[80:83]}"(acc), "+{v[84:87]}"(s), "+{v[88:91]}"(pc)
                 : [v4] "v"(vv[4]), [v5] "v"(vv[5]), [v6] "v"(vv[6]), [v7] "v"(vv[7]), [p4] "v"(p4), [p5] "v"(p5), [p6] "v"(p6), [p7] "v"(p7),
                   [f] "v"(f), [xo] "v"(x2o), [oa] "v"(oa), [sig] "v"(sig), [one] "v"(one), [rs] "s"(rs), [aw] "s"(aw)
                 : "memory", "scc", "vcc", "v92", "v93", "v94", "v95", "v96", "v97", "v98", "v99", "v100");
    asm volatile(".Lcode_end:");
    __builtin_amdgcn_s_waitcnt(0);
  }
}

extern "C" void kernel_launch(void* const* d_in, const int* in_sizes, int n_in, void* d_out, int out_size, void* d_ws, size_t ws_size,
                              hipStream_t stream) {
  fused_decode_kernel<<<256, NTHR, LDS_BYTES, stream>>>(
      (const char*)d_in[0], (const char*)d_in[1], (const char*)d_in[2], (const char*)d_in[3], (const char*)d_in[4],
      (const char*)d_in[6], (const char*)d_in[7], (const char*)d_in[8], (char*)d_out, (char*)d_ws);
}
```
